```python
import torch

def _softmax(x, axis=-1):
    return torch.softmax(x, dim=axis)
def _gelu(x):
    return torch.nn.functional.gelu(x, approximate="tanh")
def _silu(x):
    return torch.nn.functional.silu(x)
def _sigmoid(x):
    return torch.sigmoid(x)
def _layernorm(x, axis=-1, eps=1e-5):
    m = x.mean(dim=axis, keepdim=True)
    v = x.var(dim=axis, keepdim=True, unbiased=False)
    return (x - m) / torch.sqrt(v + eps)
def _scatter_add(tgt, idx, src, axis=0):
    return tgt.clone().index_add_(axis, idx, src)
def _segment_sum(src, seg, n):
    out = torch.zeros((n,) + src.shape[1:], dtype=src.dtype)
    return out.index_add_(0, seg, src)
def _pad(x, pw):
    flat = tuple(v for pair in reversed(pw) for v in pair)
    return torch.nn.functional.pad(x, flat)

kernel_name = 'standalone__gqa_softmax__near_ridge'

def setup_inputs():

    _w = (lambda a: (lambda t: t.to(torch.bfloat16) if t.is_floating_point() else t)(torch.as_tensor(a)))
    B, Hq, Hkv, Sq, Sk, D = 16, 16, 8, 384, 384, 256
    return {
        "q": _w((lambda *s: torch.randn(*s, dtype=torch.bfloat16) * 0.8)(B, Hq, Sq, D)),
        "k": _w((lambda *s: torch.randn(*s, dtype=torch.bfloat16) * 0.8)(B, Hkv, Sk, D)),
        "v": _w((lambda *s: torch.randn(*s, dtype=torch.bfloat16) * 0.05)(B, Hkv, Sk, D)),
    }

def reference(q, k, v):
    B, Hq, Sq, D = q.shape
    Hkv = k.shape[1]
    rep = Hq // Hkv
    k = (lambda x, n, axis: torch.repeat_interleave(x, n, dim=axis))(k, rep, axis=1)
    v = (lambda x, n, axis: torch.repeat_interleave(x, n, dim=axis))(v, rep, axis=1)
    scale = D ** -0.5
    scores = torch.einsum("bhqd,bhkd->bhqk", q, k) * scale
    p = _softmax(scores, axis=-1)
    return torch.einsum("bhqk,bhkd->bhqd", p, v)
```

```cpp
#include <hip/hip_runtime.h>
#include <hip/hip_bf16.h>
typedef short bf16x8 __attribute__((ext_vector_type(8)));
typedef short s16x4 __attribute__((ext_vector_type(4)));
typedef float f32x16 __attribute__((ext_vector_type(16)));
typedef unsigned u32x4 __attribute__((ext_vector_type(4)));
typedef unsigned u32x2 __attribute__((ext_vector_type(2)));
constexpr int S = 384, D = 256;
__device__ __forceinline__ unsigned cvtpk(float lo, float hi) { unsigned r; asm volatile("v_cvt_pk_bf16_f32 %0, %1, %2" : "=v"(r) : "v"(lo), "v"(hi)); return r; }
__global__ __launch_bounds__(256) void attn_base(const unsigned short* __restrict__ q, const unsigned short* __restrict__ k, const unsigned short* __restrict__ v, unsigned short* __restrict__ o) {
    __shared__ __attribute__((aligned(16))) char lds[2 * 32 * 528];
    char* ks = lds; char* vs = lds + 32 * 528;
    int tid = threadIdx.x, lane = tid & 63, w = tid >> 6, r = lane & 31, h = lane >> 5;
    int blk = blockIdx.x; int bh = blk / 3, qb = blk % 3; int b = bh >> 4, hq = bh & 15, kvh = hq >> 1;
    const unsigned short* qp = q + ((size_t)bh * S + qb * 128 + w * 32 + r) * D;
    const unsigned short* kp = k + (size_t)(b * 8 + kvh) * S * D;
    const unsigned short* vp = v + (size_t)(b * 8 + kvh) * S * D;
    bf16x8 qf[16];
#pragma unroll
    for (int s = 0; s < 16; ++s) qf[s] = *(const bf16x8*)(qp + s * 16 + h * 8);
    f32x16 acc[8];
#pragma unroll
    for (int t = 0; t < 8; ++t) acc[t] = f32x16{};
    float m = -1e30f, l = 0.f;
    const float C = 0.0625f * 1.4426950408889634f;
    for (int kt = 0; kt < 12; ++kt) {
        __syncthreads();
#pragma unroll
        for (int i = 0; i < 4; ++i) { int c = tid + i * 256; int row = c >> 5, ch = c & 31;
            *(u32x4*)(ks + row * 528 + ch * 16) = *(const u32x4*)(kp + (size_t)(kt * 32 + row) * D + ch * 8);
            *(u32x4*)(vs + row * 528 + ch * 16) = *(const u32x4*)(vp + (size_t)(kt * 32 + row) * D + ch * 8); }
        __syncthreads();
        f32x16 sacc = f32x16{};
#pragma unroll
        for (int s = 0; s < 16; ++s) { bf16x8 kf = *(const bf16x8*)(ks + r * 528 + s * 32 + h * 16); sacc = __builtin_amdgcn_mfma_f32_32x32x16_bf16(kf, qf[s], sacc, 0, 0, 0); }
        float mx = sacc[0];
#pragma unroll
        for (int i = 1; i < 16; ++i) mx = fmaxf(mx, sacc[i]);
        mx = fmaxf(mx, __shfl_xor(mx, 32));
        float mn = fmaxf(m, mx); float al = __builtin_amdgcn_exp2f((m - mn) * C); m = mn;
        float ps = 0;
#pragma unroll
        for (int i = 0; i < 16; ++i) { sacc[i] = __builtin_amdgcn_exp2f(sacc[i] * C - mn * C); ps += sacc[i]; }
        ps += __shfl_xor(ps, 32); l = l * al + ps;
        u32x4 p0 = {cvtpk(sacc[0], sacc[1]), cvtpk(sacc[2], sacc[3]), cvtpk(sacc[4], sacc[5]), cvtpk(sacc[6], sacc[7])};
        u32x4 p1 = {cvtpk(sacc[8], sacc[9]), cvtpk(sacc[10], sacc[11]), cvtpk(sacc[12], sacc[13]), cvtpk(sacc[14], sacc[15])};
        bf16x8 pf0 = __builtin_bit_cast(bf16x8, p0), pf1 = __builtin_bit_cast(bf16x8, p1);
#pragma unroll
        for (int t = 0; t < 8; ++t) {
#pragma unroll
            for (int i = 0; i < 16; ++i) acc[t][i] *= al;
            bf16x8 a0, a1;
#pragma unroll
            for (int j = 0; j < 8; ++j) { int key = 8 * (j >> 2) + 4 * h + (j & 3);
                a0[j] = *(const short*)(vs + key * 528 + (32 * t + r) * 2); a1[j] = *(const short*)(vs + (16 + key) * 528 + (32 * t + r) * 2); }
            acc[t] = __builtin_amdgcn_mfma_f32_32x32x16_bf16(a0, pf0, acc[t], 0, 0, 0);
            acc[t] = __builtin_amdgcn_mfma_f32_32x32x16_bf16(a1, pf1, acc[t], 0, 0, 0);
        }
    }
    float il = 1.f / l;
    unsigned short* op = o + ((size_t)bh * S + qb * 128 + w * 32 + r) * D;
#pragma unroll
    for (int t = 0; t < 8; ++t)
#pragma unroll
        for (int g = 0; g < 4; ++g) { u32x2 x = {cvtpk(acc[t][4 * g] * il, acc[t][4 * g + 1] * il), cvtpk(acc[t][4 * g + 2] * il, acc[t][4 * g + 3] * il)};
            *(u32x2*)(op + 32 * t + 8 * g + 4 * h) = x; }
}
extern "C" void kernel_launch(void* const* d_in, const int* in_sizes, int n_in, void* d_out, int out_size, void* d_ws, size_t ws_size, hipStream_t stream) {
    attn_base<<<768, 256, 0, stream>>>((const unsigned short*)d_in[0], (const unsigned short*)d_in[1], (const unsigned short*)d_in[2], (unsigned short*)d_out);
}
```

```cpp
#include <hip/hip_runtime.h>

__global__ __launch_bounds__(256, 1) void gqa_softmax_kernel(const void* q, const void* k, const void* v, void* o) {
    __shared__ __attribute__((aligned(16))) char lds[163840];
    const unsigned wave = __builtin_amdgcn_readfirstlane(threadIdx.x >> 6);
    asm volatile(
        "s_mov_b32 s63, %5\n"
        "s_and_b32 s64, %4, 7\n"
        "s_lshr_b32 s65, %4, 3\n"
        "s_lshl_b32 s64, s64, 4\n"
        "s_lshr_b32 s66, s65, 1\n"
        "s_add_u32 s64, s64, s66\n"
        "s_and_b32 s65, s65, 1\n"
        "s_lshl_b32 s66, s64, 1\n"
        "s_add_u32 s65, s65, s66\n"
        "s_mul_i32 s64, s64, 196608\n"
        "s_mul_i32 s65, s65, 196608\n"
        "s_mov_b64 s[40:41], %1\n"
        "s_add_u32 s40, s40, s64\n"
        "s_addc_u32 s41, s41, 0\n"
        "s_and_b32 s41, s41, 0xffff\n"
        "s_mov_b32 s42, 196608\n"
        "s_mov_b32 s43, 0x20000\n"
        "s_mov_b64 s[36:37], %0\n"
        "s_add_u32 s36, s36, s65\n"
        "s_addc_u32 s37, s37, 0\n"
        "s_and_b32 s37, s37, 0xffff\n"
        "s_mov_b32 s38, 196608\n"
        "s_mov_b32 s39, 0x20000\n"
        "s_mov_b64 s[44:45], %2\n"
        "s_add_u32 s44, s44, s64\n"
        "s_addc_u32 s45, s45, 0\n"
        "s_and_b32 s45, s45, 0xffff\n"
        "s_mov_b32 s46, 196608\n"
        "s_mov_b32 s47, 0x20000\n"
        "s_mov_b64 s[48:49], %3\n"
        "s_add_u32 s48, s48, s65\n"
        "s_addc_u32 s49, s49, 0\n"
        "s_and_b32 s49, s49, 0xffff\n"
        "s_mov_b32 s50, 196608\n"
        "s_mov_b32 s51, 0x20000\n"
        "v_mbcnt_lo_u32_b32 v0, -1, 0\n"
        "v_mbcnt_hi_u32_b32 v0, -1, v0\n"
        "v_bfe_u32 v1, v0, 3, 2\n"
        "v_lshlrev_b32 v1, 11, v1\n"
        "v_lshrrev_b32 v2, 5, v0\n"
        "v_lshl_add_u32 v1, v2, 8, v1\n"
        "v_and_b32 v2, 7, v0\n"
        "v_lshl_add_u32 v1, v2, 4, v1\n"
        "v_and_b32 v3, 16, v0\n"
        "v_xor_b32 v48, v1, v3\n"
        "s_mov_b32 s62, s63\n"
        "s_mul_i32 s71, s62, 49152\n"
        "v_lshrrev_b32 v1, 5, v0\n"
        "v_lshlrev_b32 v1, 13, v1\n"
        "v_and_b32 v2, 31, v0\n"
        "v_lshl_add_u32 v117, v2, 4, v1\n"
        "s_mul_i32 s67, s63, 13824\n"
        "s_add_u32 s67, s67, 108288\n"
        "s_mul_i32 s70, s63, 14592\n"
        "s_mul_i32 s69, s63, 4160\n"
        "s_lshl_b32 s54, s63, 11\n"
        "s_add_u32 m0, s69, 58368\n"
        "s_nop 0\n"
        "buffer_load_dwordx4 v117, s[36:39], s54 offen nt lds\n"
        "s_add_u32 m0, s69, 58896\n"
        "s_nop 0\n"
        "buffer_load_dwordx4 v117, s[36:39], s54 offen offset:512 nt lds\n"
        "s_add_u32 m0, s69, 59424\n"
        "s_nop 0\n"
        "buffer_load_dwordx4 v117, s[36:39], s54 offen offset:1024 nt lds\n"
        "s_add_u32 m0, s69, 59952\n"
        "s_nop 0\n"
        "buffer_load_dwordx4 v117, s[36:39], s54 offen offset:1536 nt lds\n"
        "s_add_u32 s54, s54, 16384\n"
        "s_add_u32 s68, s71, 0\n"
        "s_add_u32 m0, s67, 0\n"
        "s_nop 0\n"
        "buffer_load_dwordx4 v48, s[40:43], s68 offen  lds\n"
        "s_add_u32 m0, s67, 544\n"
        "s_nop 0\n"
        "buffer_load_dwordx4 v48, s[40:43], s68 offen offset:512  lds\n"
        "s_add_u32 m0, s67, 1088\n"
        "s_nop 0\n"
        "buffer_load_dwordx4 v48, s[40:43], s68 offen offset:1024  lds\n"
        "s_add_u32 m0, s67, 1632\n"
        "s_nop 0\n"
        "buffer_load_dwordx4 v48, s[40:43], s68 offen offset:1536  lds\n"
        "s_getpc_b64 s[72:73]\n"
        "5:\n"
        "s_and_b32 s73, s73, 0xffff\n"
        "s_mov_b32 s74, 9f-5b\n"
        "s_mov_b32 s75, 0x20000\n"
        "s_lshr_b32 s64, %4, 3\n"
        "s_and_b32 s64, s64, 7\n"
        "s_lshl_b32 s64, s64, 2\n"
        "s_add_u32 s64, s64, %5\n"
        "s_lshl_b32 s64, s64, 10\n"
        "v_lshlrev_b32 v56, 4, v0\n"
        "buffer_load_dwordx4 v[52:55], v56, s[72:75], s64 offen\n"
        "s_add_u32 s68, s71, 128\n"
        "s_add_u32 m0, s67, 4224\n"
        "s_nop 0\n"
        "buffer_load_dwordx4 v48, s[40:43], s68 offen  lds\n"
        "s_add_u32 m0, s67, 4768\n"
        "s_nop 0\n"
        "buffer_load_dwordx4 v48, s[40:43], s68 offen offset:512  lds\n"
        "s_add_u32 m0, s67, 5312\n"
        "s_nop 0\n"
        "buffer_load_dwordx4 v48, s[40:43], s68 offen offset:1024  lds\n"
        "s_add_u32 m0, s67, 5856\n"
        "s_nop 0\n"
        "buffer_load_dwordx4 v48, s[40:43], s68 offen offset:1536  lds\n"
        "s_add_u32 s68, s71, 8192\n"
        "s_add_u32 m0, s67, 8448\n"
        "s_nop 0\n"
        "buffer_load_dwordx4 v48, s[40:43], s68 offen  lds\n"
        "s_add_u32 m0, s67, 8992\n"
        "s_nop 0\n"
        "buffer_load_dwordx4 v48, s[40:43], s68 offen offset:512  lds\n"
        "s_add_u32 m0, s67, 9536\n"
        "s_nop 0\n"
        "buffer_load_dwordx4 v48, s[40:43], s68 offen offset:1024  lds\n"
        "s_add_u32 m0, s67, 10080\n"
        "s_nop 0\n"
        "buffer_load_dwordx4 v48, s[40:43], s68 offen offset:1536  lds\n"
        "v_lshrrev_b32 v1, 3, v0\n"
        "v_lshlrev_b32 v1, 11, v1\n"
        "v_and_b32 v2, 7, v0\n"
        "v_lshl_add_u32 v114, v2, 4, v1\n"
        "s_lshl_b32 s65, s63, 7\n"
        "s_add_u32 s64, s62, 0\n"
        "s_and_b32 s64, s64, 3\n"
        "s_mul_i32 s64, s64, 49152\n"
        "s_add_u32 s72, s64, s65\n"
        "s_add_u32 s64, s62, 1\n"
        "s_and_b32 s64, s64, 3\n"
        "s_mul_i32 s64, s64, 49152\n"
        "s_add_u32 s73, s64, s65\n"
        "s_add_u32 s64, s62, 2\n"
        "s_and_b32 s64, s64, 3\n"
        "s_mul_i32 s64, s64, 49152\n"
        "s_add_u32 s74, s64, s65\n"
        "s_add_u32 s64, s62, 3\n"
        "s_and_b32 s64, s64, 3\n"
        "s_mul_i32 s64, s64, 49152\n"
        "s_add_u32 s75, s64, s65\n"
        "v_and_b32 v4, 15, v0\n"
        "v_lshrrev_b32 v5, 4, v0\n"
        "v_mul_u32_u24 v1, 1040, v4\n"
        "v_lshrrev_b32 v2, 1, v5\n"
        "v_lshl_add_u32 v1, v2, 4, v1\n"
        "v_and_b32 v2, 1, v5\n"
        "v_lshl_add_u32 v1, v2, 8, v1\n"
        "v_add_u32 v108, 58368, v1\n"
        "s_mul_i32 s64, s62, 6144\n"
        "s_add_u32 s64, s64, 32768\n"
        "v_lshlrev_b32 v1, 4, v0\n"
        "v_add_u32 v109, s64, v1\n"
        "v_mov_b32 v110, v109\n"
        "s_lshl_b32 s64, s63, 2\n"
        "s_add_u32 s64, s64, 24576\n"
        "v_lshlrev_b32 v1, 4, v4\n"
        "v_add_u32 v113, s64, v1\n"
        "v_add_u32 v112, 57344, v1\n"
        "v_lshrrev_b32 v2, 5, v0\n"
        "v_lshl_add_u32 v111, v2, 8, v113\n"
        "v_and_b32 v1, 3, v4\n"
        "v_mul_u32_u24 v1, 1056, v1\n"
        "v_lshrrev_b32 v2, 2, v4\n"
        "v_lshl_add_u32 v1, v2, 7, v1\n"
        "v_and_b32 v2, 1, v5\n"
        "v_lshl_add_u32 v1, v2, 9, v1\n"
        "v_lshrrev_b32 v2, 3, v4\n"
        "v_lshrrev_b32 v3, 1, v5\n"
        "v_xor_b32 v2, v2, v3\n"
        "v_lshl_add_u32 v1, v2, 4, v1\n"
        "v_add_u32 v49, s67, v1\n"
        "v_add_u32 v50, s70, v1\n"
        "v_bfe_u32 v1, v0, 2, 2\n"
        "v_mul_u32_u24 v1, 1056, v1\n"
        "v_lshl_add_u32 v1, v5, 7, v1\n"
        "v_and_b32 v2, 3, v0\n"
        "v_lshl_add_u32 v1, v2, 3, v1\n"
        "v_add_u32 v115, s67, v1\n"
        "v_add_u32 v116, s70, v1\n"
        "v_mov_b32 v124, 0x3f803f80\n"
        "v_mov_b32 v125, 0x3f803f80\n"
        "v_mov_b32 v126, 0x3f803f80\n"
        "v_mov_b32 v127, 0x3f803f80\n"
        "s_mov_b32 s52, 0x3db8aa3b\n"
        "s_lshl_b32 s56, s63, 7\n"
        "s_add_u32 s64, s62, 1\n"
        "s_and_b32 s64, s64, 3\n"
        "s_cmp_eq_u32 s64, 0\n"
        "s_cselect_b32 s57, -3, 1\n"
        "s_mul_i32 s57, s57, 6144\n"
        "s_add_u32 s64, s62, 2\n"
        "s_and_b32 s64, s64, 3\n"
        "s_cmp_eq_u32 s64, 0\n"
        "s_cselect_b32 s58, -3, 1\n"
        "s_mul_i32 s58, s58, 6144\n"
        "s_add_u32 s64, s62, 3\n"
        "s_and_b32 s64, s64, 3\n"
        "s_cmp_eq_u32 s64, 0\n"
        "s_cselect_b32 s59, -3, 1\n"
        "s_mul_i32 s59, s59, 6144\n"
        "s_add_u32 s60, s58, s59\n"
        "s_sub_u32 s60, 0, s60\n"
        "s_mov_b32 s61, 58368\n"
        "s_mov_b32 s55, 58368\n"
        "s_mov_b32 s53, 8\n"
        "s_waitcnt vmcnt(9)\n"
        "ds_read_b128 a[0:3], v49 offset:0\n"
        "ds_read_b128 a[4:7], v49 offset:32\n"
        "ds_read_b128 a[8:11], v49 offset:64\n"
        "ds_read_b128 a[12:15], v49 offset:96\n"
        "s_waitcnt lgkmcnt(0)\n"
        "s_add_u32 s68, s71, 8320\n"
        "s_add_u32 m0, s67, 0\n"
        "s_nop 0\n"
        "buffer_load_dwordx4 v48, s[40:43], s68 offen  lds\n"
        "s_add_u32 m0, s67, 544\n"
        "s_nop 0\n"
        "buffer_load_dwordx4 v48, s[40:43], s68 offen offset:512  lds\n"
        "s_add_u32 m0, s67, 1088\n"
        "s_nop 0\n"
        "buffer_load_dwordx4 v48, s[40:43], s68 offen offset:1024  lds\n"
        "s_add_u32 m0, s67, 1632\n"
        "s_nop 0\n"
        "buffer_load_dwordx4 v48, s[40:43], s68 offen offset:1536  lds\n"
        "s_waitcnt vmcnt(8)\n"
        "ds_read_b128 a[16:19], v49 offset:4224\n"
        "ds_read_b128 a[20:23], v49 offset:4256\n"
        "ds_read_b128 a[24:27], v49 offset:4288\n"
        "ds_read_b128 a[28:31], v49 offset:4320\n"
        "s_waitcnt lgkmcnt(0)\n"
        "s_add_u32 s68, s71, 16384\n"
        "s_add_u32 m0, s67, 4224\n"
        "s_nop 0\n"
        "buffer_load_dwordx4 v48, s[40:43], s68 offen  lds\n"
        "s_add_u32 m0, s67, 4768\n"
        "s_nop 0\n"
        "buffer_load_dwordx4 v48, s[40:43], s68 offen offset:512  lds\n"
        "s_add_u32 m0, s67, 5312\n"
        "s_nop 0\n"
        "buffer_load_dwordx4 v48, s[40:43], s68 offen offset:1024  lds\n"
        "s_add_u32 m0, s67, 5856\n"
        "s_nop 0\n"
        "buffer_load_dwordx4 v48, s[40:43], s68 offen offset:1536  lds\n"
        "s_waitcnt vmcnt(21) lgkmcnt(0)\n"
        "s_barrier\n"
        "ds_read_b128 v[176:179], v108 offset:0\n"
        "ds_read_b128 v[180:183], v108 offset:32\n"
        "ds_read_b128 v[184:187], v108 offset:64\n"
        "ds_read_b128 v[188:191], v108 offset:96\n"
        "ds_read_b128 v[192:195], v108 offset:128\n"
        "ds_read_b128 v[196:199], v108 offset:160\n"
        "ds_read_b128 v[200:203], v108 offset:192\n"
        "ds_read_b128 v[204:207], v108 offset:224\n"
        "ds_read_b128 v[208:211], v108 offset:512\n"
        "ds_read_b128 v[212:215], v108 offset:544\n"
        "ds_read_b128 v[216:219], v108 offset:576\n"
        "ds_read_b128 v[220:223], v108 offset:608\n"
        "ds_read_b128 v[224:227], v108 offset:640\n"
        "ds_read_b128 v[228:231], v108 offset:672\n"
        "ds_read_b128 v[232:235], v108 offset:704\n"
        "ds_read_b128 v[236:239], v108 offset:736\n"
        "s_waitcnt lgkmcnt(0)\n"
        "v_mfma_f32_16x16x32_bf16 v[0:3], a[0:3], v[176:179], 0\n"
        "v_mfma_f32_16x16x32_bf16 v[0:3], a[4:7], v[180:183], v[0:3]\n"
        "v_mfma_f32_16x16x32_bf16 v[0:3], a[8:11], v[184:187], v[0:3]\n"
        "v_mfma_f32_16x16x32_bf16 v[0:3], a[12:15], v[188:191], v[0:3]\n"
        "v_mfma_f32_16x16x32_bf16 v[0:3], a[16:19], v[192:195], v[0:3]\n"
        "v_mfma_f32_16x16x32_bf16 v[0:3], a[20:23], v[196:199], v[0:3]\n"
        "v_mfma_f32_16x16x32_bf16 v[0:3], a[24:27], v[200:203], v[0:3]\n"
        "v_mfma_f32_16x16x32_bf16 v[0:3], a[28:31], v[204:207], v[0:3]\n"
        "v_mfma_f32_16x16x32_bf16 v[24:27], a[0:3], v[208:211], 0\n"
        "v_mfma_f32_16x16x32_bf16 v[24:27], a[4:7], v[212:215], v[24:27]\n"
        "v_mfma_f32_16x16x32_bf16 v[24:27], a[8:11], v[216:219], v[24:27]\n"
        "v_mfma_f32_16x16x32_bf16 v[24:27], a[12:15], v[220:223], v[24:27]\n"
        "v_mfma_f32_16x16x32_bf16 v[24:27], a[16:19], v[224:227], v[24:27]\n"
        "v_mfma_f32_16x16x32_bf16 v[24:27], a[20:23], v[228:231], v[24:27]\n"
        "v_mfma_f32_16x16x32_bf16 v[24:27], a[24:27], v[232:235], v[24:27]\n"
        "v_mfma_f32_16x16x32_bf16 v[24:27], a[28:31], v[236:239], v[24:27]\n"
        "s_waitcnt vmcnt(8)\n"
        "ds_read_b128 a[32:35], v49 offset:8448\n"
        "ds_read_b128 a[36:39], v49 offset:8480\n"
        "ds_read_b128 a[40:43], v49 offset:8512\n"
        "ds_read_b128 a[44:47], v49 offset:8544\n"
        "s_waitcnt lgkmcnt(0)\n"
        "s_add_u32 s68, s71, 16512\n"
        "s_add_u32 m0, s67, 8448\n"
        "s_nop 0\n"
        "buffer_load_dwordx4 v48, s[40:43], s68 offen  lds\n"
        "s_add_u32 m0, s67, 8992\n"
        "s_nop 0\n"
        "buffer_load_dwordx4 v48, s[40:43], s68 offen offset:512  lds\n"
        "s_add_u32 m0, s67, 9536\n"
        "s_nop 0\n"
        "buffer_load_dwordx4 v48, s[40:43], s68 offen offset:1024  lds\n"
        "s_add_u32 m0, s67, 10080\n"
        "s_nop 0\n"
        "buffer_load_dwordx4 v48, s[40:43], s68 offen offset:1536  lds\n"
        "s_waitcnt vmcnt(8)\n"
        "ds_read_b128 a[48:51], v49 offset:0\n"
        "ds_read_b128 a[52:55], v49 offset:32\n"
        "ds_read_b128 a[56:59], v49 offset:64\n"
        "ds_read_b128 a[60:63], v49 offset:96\n"
        "s_waitcnt lgkmcnt(0)\n"
        "s_add_u32 s68, s71, 24576\n"
        "s_add_u32 m0, s67, 0\n"
        "s_nop 0\n"
        "buffer_load_dwordx4 v48, s[40:43], s68 offen  lds\n"
        "s_add_u32 m0, s67, 544\n"
        "s_nop 0\n"
        "buffer_load_dwordx4 v48, s[40:43], s68 offen offset:512  lds\n"
        "s_add_u32 m0, s67, 1088\n"
        "s_nop 0\n"
        "buffer_load_dwordx4 v48, s[40:43], s68 offen offset:1024  lds\n"
        "s_add_u32 m0, s67, 1632\n"
        "s_nop 0\n"
        "buffer_load_dwordx4 v48, s[40:43], s68 offen offset:1536  lds\n"
        "v_mfma_f32_16x16x32_bf16 v[4:7], a[32:35], v[176:179], 0\n"
        "v_mfma_f32_16x16x32_bf16 v[4:7], a[36:39], v[180:183], v[4:7]\n"
        "v_mfma_f32_16x16x32_bf16 v[4:7], a[40:43], v[184:187], v[4:7]\n"
        "v_mfma_f32_16x16x32_bf16 v[4:7], a[44:47], v[188:191], v[4:7]\n"
        "v_max_f32 v118, v0, v1\n"
        "v_max3_f32 v118, v118, v2, v3\n"
        "v_max_f32 v119, v24, v25\n"
        "v_max3_f32 v119, v119, v26, v27\n"
        "v_mfma_f32_16x16x32_bf16 v[4:7], a[48:51], v[192:195], v[4:7]\n"
        "v_mfma_f32_16x16x32_bf16 v[4:7], a[52:55], v[196:199], v[4:7]\n"
        "v_mfma_f32_16x16x32_bf16 v[4:7], a[56:59], v[200:203], v[4:7]\n"
        "v_mfma_f32_16x16x32_bf16 v[4:7], a[60:63], v[204:207], v[4:7]\n"
        "v_mfma_f32_16x16x32_bf16 v[28:31], a[32:35], v[208:211], 0\n"
        "v_mfma_f32_16x16x32_bf16 v[28:31], a[36:39], v[212:215], v[28:31]\n"
        "v_mfma_f32_16x16x32_bf16 v[28:31], a[40:43], v[216:219], v[28:31]\n"
        "v_mfma_f32_16x16x32_bf16 v[28:31], a[44:47], v[220:223], v[28:31]\n"
        "v_mfma_f32_16x16x32_bf16 v[28:31], a[48:51], v[224:227], v[28:31]\n"
        "v_mfma_f32_16x16x32_bf16 v[28:31], a[52:55], v[228:231], v[28:31]\n"
        "v_mfma_f32_16x16x32_bf16 v[28:31], a[56:59], v[232:235], v[28:31]\n"
        "v_mfma_f32_16x16x32_bf16 v[28:31], a[60:63], v[236:239], v[28:31]\n"
        "s_waitcnt vmcnt(8)\n"
        "ds_read_b128 a[64:67], v49 offset:4224\n"
        "ds_read_b128 a[68:71], v49 offset:4256\n"
        "ds_read_b128 a[72:75], v49 offset:4288\n"
        "ds_read_b128 a[76:79], v49 offset:4320\n"
        "s_waitcnt lgkmcnt(0)\n"
        "s_add_u32 s68, s71, 24704\n"
        "s_add_u32 m0, s67, 4224\n"
        "s_nop 0\n"
        "buffer_load_dwordx4 v48, s[40:43], s68 offen  lds\n"
        "s_add_u32 m0, s67, 4768\n"
        "s_nop 0\n"
        "buffer_load_dwordx4 v48, s[40:43], s68 offen offset:512  lds\n"
        "s_add_u32 m0, s67, 5312\n"
        "s_nop 0\n"
        "buffer_load_dwordx4 v48, s[40:43], s68 offen offset:1024  lds\n"
        "s_add_u32 m0, s67, 5856\n"
        "s_nop 0\n"
        "buffer_load_dwordx4 v48, s[40:43], s68 offen offset:1536  lds\n"
        "s_waitcnt vmcnt(8)\n"
        "ds_read_b128 a[80:83], v49 offset:8448\n"
        "ds_read_b128 a[84:87], v49 offset:8480\n"
        "ds_read_b128 a[88:91], v49 offset:8512\n"
        "ds_read_b128 a[92:95], v49 offset:8544\n"
        "s_waitcnt lgkmcnt(0)\n"
        "s_add_u32 s68, s71, 32768\n"
        "s_add_u32 m0, s67, 8448\n"
        "s_nop 0\n"
        "buffer_load_dwordx4 v48, s[40:43], s68 offen  lds\n"
        "s_add_u32 m0, s67, 8992\n"
        "s_nop 0\n"
        "buffer_load_dwordx4 v48, s[40:43], s68 offen offset:512  lds\n"
        "s_add_u32 m0, s67, 9536\n"
        "s_nop 0\n"
        "buffer_load_dwordx4 v48, s[40:43], s68 offen offset:1024  lds\n"
        "s_add_u32 m0, s67, 10080\n"
        "s_nop 0\n"
        "buffer_load_dwordx4 v48, s[40:43], s68 offen offset:1536  lds\n"
        "v_mfma_f32_16x16x32_bf16 v[8:11], a[64:67], v[176:179], 0\n"
        "v_mfma_f32_16x16x32_bf16 v[8:11], a[68:71], v[180:183], v[8:11]\n"
        "v_mfma_f32_16x16x32_bf16 v[8:11], a[72:75], v[184:187], v[8:11]\n"
        "v_mfma_f32_16x16x32_bf16 v[8:11], a[76:79], v[188:191], v[8:11]\n"
        "v_max3_f32 v118, v118, v4, v5\n"
        "v_max3_f32 v118, v118, v6, v7\n"
        "v_max3_f32 v119, v119, v28, v29\n"
        "v_max3_f32 v119, v119, v30, v31\n"
        "v_mfma_f32_16x16x32_bf16 v[8:11], a[80:83], v[192:195], v[8:11]\n"
        "v_mfma_f32_16x16x32_bf16 v[8:11], a[84:87], v[196:199], v[8:11]\n"
        "v_mfma_f32_16x16x32_bf16 v[8:11], a[88:91], v[200:203], v[8:11]\n"
        "v_mfma_f32_16x16x32_bf16 v[8:11], a[92:95], v[204:207], v[8:11]\n"
        "v_mfma_f32_16x16x32_bf16 v[32:35], a[64:67], v[208:211], 0\n"
        "v_mfma_f32_16x16x32_bf16 v[32:35], a[68:71], v[212:215], v[32:35]\n"
        "v_mfma_f32_16x16x32_bf16 v[32:35], a[72:75], v[216:219], v[32:35]\n"
        "v_mfma_f32_16x16x32_bf16 v[32:35], a[76:79], v[220:223], v[32:35]\n"
        "v_mfma_f32_16x16x32_bf16 v[32:35], a[80:83], v[224:227], v[32:35]\n"
        "v_mfma_f32_16x16x32_bf16 v[32:35], a[84:87], v[228:231], v[32:35]\n"
        "v_mfma_f32_16x16x32_bf16 v[32:35], a[88:91], v[232:235], v[32:35]\n"
        "v_mfma_f32_16x16x32_bf16 v[32:35], a[92:95], v[236:239], v[32:35]\n"
        "s_waitcnt vmcnt(8)\n"
        "ds_read_b128 a[96:99], v49 offset:0\n"
        "ds_read_b128 a[100:103], v49 offset:32\n"
        "ds_read_b128 a[104:107], v49 offset:64\n"
        "ds_read_b128 a[108:111], v49 offset:96\n"
        "s_waitcnt lgkmcnt(0)\n"
        "s_add_u32 s68, s71, 32896\n"
        "s_add_u32 m0, s67, 0\n"
        "s_nop 0\n"
        "buffer_load_dwordx4 v48, s[40:43], s68 offen  lds\n"
        "s_add_u32 m0, s67, 544\n"
        "s_nop 0\n"
        "buffer_load_dwordx4 v48, s[40:43], s68 offen offset:512  lds\n"
        "s_add_u32 m0, s67, 1088\n"
        "s_nop 0\n"
        "buffer_load_dwordx4 v48, s[40:43], s68 offen offset:1024  lds\n"
        "s_add_u32 m0, s67, 1632\n"
        "s_nop 0\n"
        "buffer_load_dwordx4 v48, s[40:43], s68 offen offset:1536  lds\n"
        "s_waitcnt vmcnt(8)\n"
        "ds_read_b128 a[112:115], v49 offset:4224\n"
        "ds_read_b128 a[116:119], v49 offset:4256\n"
        "ds_read_b128 a[120:123], v49 offset:4288\n"
        "ds_read_b128 a[124:127], v49 offset:4320\n"
        "s_waitcnt lgkmcnt(0)\n"
        "s_add_u32 s68, s71, 40960\n"
        "s_add_u32 m0, s67, 4224\n"
        "s_nop 0\n"
        "buffer_load_dwordx4 v48, s[40:43], s68 offen  lds\n"
        "s_add_u32 m0, s67, 4768\n"
        "s_nop 0\n"
        "buffer_load_dwordx4 v48, s[40:43], s68 offen offset:512  lds\n"
        "s_add_u32 m0, s67, 5312\n"
        "s_nop 0\n"
        "buffer_load_dwordx4 v48, s[40:43], s68 offen offset:1024  lds\n"
        "s_add_u32 m0, s67, 5856\n"
        "s_nop 0\n"
        "buffer_load_dwordx4 v48, s[40:43], s68 offen offset:1536  lds\n"
        "v_mfma_f32_16x16x32_bf16 v[12:15], a[96:99], v[176:179], 0\n"
        "v_mfma_f32_16x16x32_bf16 v[12:15], a[100:103], v[180:183], v[12:15]\n"
        "v_mfma_f32_16x16x32_bf16 v[12:15], a[104:107], v[184:187], v[12:15]\n"
        "v_mfma_f32_16x16x32_bf16 v[12:15], a[108:111], v[188:191], v[12:15]\n"
        "v_max3_f32 v118, v118, v8, v9\n"
        "v_max3_f32 v118, v118, v10, v11\n"
        "v_max3_f32 v119, v119, v32, v33\n"
        "v_max3_f32 v119, v119, v34, v35\n"
        "v_mfma_f32_16x16x32_bf16 v[12:15], a[112:115], v[192:195], v[12:15]\n"
        "v_mfma_f32_16x16x32_bf16 v[12:15], a[116:119], v[196:199], v[12:15]\n"
        "v_mfma_f32_16x16x32_bf16 v[12:15], a[120:123], v[200:203], v[12:15]\n"
        "v_mfma_f32_16x16x32_bf16 v[12:15], a[124:127], v[204:207], v[12:15]\n"
        "v_mfma_f32_16x16x32_bf16 v[36:39], a[96:99], v[208:211], 0\n"
        "v_mfma_f32_16x16x32_bf16 v[36:39], a[100:103], v[212:215], v[36:39]\n"
        "v_mfma_f32_16x16x32_bf16 v[36:39], a[104:107], v[216:219], v[36:39]\n"
        "v_mfma_f32_16x16x32_bf16 v[36:39], a[108:111], v[220:223], v[36:39]\n"
        "v_mfma_f32_16x16x32_bf16 v[36:39], a[112:115], v[224:227], v[36:39]\n"
        "v_mfma_f32_16x16x32_bf16 v[36:39], a[116:119], v[228:231], v[36:39]\n"
        "v_mfma_f32_16x16x32_bf16 v[36:39], a[120:123], v[232:235], v[36:39]\n"
        "v_mfma_f32_16x16x32_bf16 v[36:39], a[124:127], v[236:239], v[36:39]\n"
        "s_waitcnt vmcnt(8)\n"
        "ds_read_b128 a[128:131], v49 offset:8448\n"
        "ds_read_b128 a[132:135], v49 offset:8480\n"
        "ds_read_b128 a[136:139], v49 offset:8512\n"
        "ds_read_b128 a[140:143], v49 offset:8544\n"
        "s_waitcnt lgkmcnt(0)\n"
        "s_add_u32 s68, s71, 41088\n"
        "s_add_u32 m0, s67, 8448\n"
        "s_nop 0\n"
        "buffer_load_dwordx4 v48, s[40:43], s68 offen  lds\n"
        "s_add_u32 m0, s67, 8992\n"
        "s_nop 0\n"
        "buffer_load_dwordx4 v48, s[40:43], s68 offen offset:512  lds\n"
        "s_add_u32 m0, s67, 9536\n"
        "s_nop 0\n"
        "buffer_load_dwordx4 v48, s[40:43], s68 offen offset:1024  lds\n"
        "s_add_u32 m0, s67, 10080\n"
        "s_nop 0\n"
        "buffer_load_dwordx4 v48, s[40:43], s68 offen offset:1536  lds\n"
        "s_waitcnt vmcnt(8)\n"
        "ds_read_b128 a[144:147], v49 offset:0\n"
        "ds_read_b128 a[148:151], v49 offset:32\n"
        "ds_read_b128 a[152:155], v49 offset:64\n"
        "ds_read_b128 a[156:159], v49 offset:96\n"
        "s_waitcnt lgkmcnt(0)\n"
        "s_add_u32 m0, s69, 75008\n"
        "s_nop 0\n"
        "buffer_load_dwordx4 v117, s[36:39], s54 offen nt lds\n"
        "s_add_u32 m0, s69, 75536\n"
        "s_nop 0\n"
        "buffer_load_dwordx4 v117, s[36:39], s54 offen offset:512 nt lds\n"
        "s_add_u32 m0, s69, 76064\n"
        "s_nop 0\n"
        "buffer_load_dwordx4 v117, s[36:39], s54 offen offset:1024 nt lds\n"
        "s_add_u32 m0, s69, 76592\n"
        "s_nop 0\n"
        "buffer_load_dwordx4 v117, s[36:39], s54 offen offset:1536 nt lds\n"
        "s_add_u32 s54, s54, 16384\n"
        "s_add_u32 s68, s72, 0\n"
        "s_add_u32 m0, s67, 0\n"
        "s_nop 0\n"
        "buffer_load_dwordx4 v114, s[44:47], s68 offen  lds\n"
        "s_add_u32 m0, s67, 544\n"
        "s_nop 0\n"
        "buffer_load_dwordx4 v114, s[44:47], s68 offen offset:512  lds\n"
        "s_add_u32 m0, s67, 1088\n"
        "s_nop 0\n"
        "buffer_load_dwordx4 v114, s[44:47], s68 offen offset:1024  lds\n"
        "s_add_u32 m0, s67, 1632\n"
        "s_nop 0\n"
        "buffer_load_dwordx4 v114, s[44:47], s68 offen offset:1536  lds\n"
        "v_mfma_f32_16x16x32_bf16 v[16:19], a[128:131], v[176:179], 0\n"
        "v_mfma_f32_16x16x32_bf16 v[16:19], a[132:135], v[180:183], v[16:19]\n"
        "v_mfma_f32_16x16x32_bf16 v[16:19], a[136:139], v[184:187], v[16:19]\n"
        "v_mfma_f32_16x16x32_bf16 v[16:19], a[140:143], v[188:191], v[16:19]\n"
        "v_max3_f32 v118, v118, v12, v13\n"
        "v_max3_f32 v118, v118, v14, v15\n"
        "v_max3_f32 v119, v119, v36, v37\n"
        "v_max3_f32 v119, v119, v38, v39\n"
        "v_mfma_f32_16x16x32_bf16 v[16:19], a[144:147], v[192:195], v[16:19]\n"
        "v_mfma_f32_16x16x32_bf16 v[16:19], a[148:151], v[196:199], v[16:19]\n"
        "v_mfma_f32_16x16x32_bf16 v[16:19], a[152:155], v[200:203], v[16:19]\n"
        "v_mfma_f32_16x16x32_bf16 v[16:19], a[156:159], v[204:207], v[16:19]\n"
        "v_mfma_f32_16x16x32_bf16 v[40:43], a[128:131], v[208:211], 0\n"
        "v_mfma_f32_16x16x32_bf16 v[40:43], a[132:135], v[212:215], v[40:43]\n"
        "v_mfma_f32_16x16x32_bf16 v[40:43], a[136:139], v[216:219], v[40:43]\n"
        "v_mfma_f32_16x16x32_bf16 v[40:43], a[140:143], v[220:223], v[40:43]\n"
        "v_mfma_f32_16x16x32_bf16 v[40:43], a[144:147], v[224:227], v[40:43]\n"
        "v_mfma_f32_16x16x32_bf16 v[40:43], a[148:151], v[228:231], v[40:43]\n"
        "v_mfma_f32_16x16x32_bf16 v[40:43], a[152:155], v[232:235], v[40:43]\n"
        "v_mfma_f32_16x16x32_bf16 v[40:43], a[156:159], v[236:239], v[40:43]\n"
        "s_waitcnt vmcnt(12)\n"
        "ds_read_b128 a[160:163], v49 offset:4224\n"
        "ds_read_b128 a[164:167], v49 offset:4256\n"
        "ds_read_b128 a[168:171], v49 offset:4288\n"
        "ds_read_b128 a[172:175], v49 offset:4320\n"
        "s_waitcnt lgkmcnt(0)\n"
        "s_add_u32 s68, s72, 16384\n"
        "s_add_u32 m0, s67, 4224\n"
        "s_nop 0\n"
        "buffer_load_dwordx4 v114, s[44:47], s68 offen  lds\n"
        "s_add_u32 m0, s67, 4768\n"
        "s_nop 0\n"
        "buffer_load_dwordx4 v114, s[44:47], s68 offen offset:512  lds\n"
        "s_add_u32 m0, s67, 5312\n"
        "s_nop 0\n"
        "buffer_load_dwordx4 v114, s[44:47], s68 offen offset:1024  lds\n"
        "s_add_u32 m0, s67, 5856\n"
        "s_nop 0\n"
        "buffer_load_dwordx4 v114, s[44:47], s68 offen offset:1536  lds\n"
        "s_waitcnt vmcnt(12)\n"
        "ds_read_b128 a[176:179], v49 offset:8448\n"
        "ds_read_b128 a[180:183], v49 offset:8480\n"
        "ds_read_b128 a[184:187], v49 offset:8512\n"
        "ds_read_b128 a[188:191], v49 offset:8544\n"
        "s_waitcnt lgkmcnt(0)\n"
        "s_add_u32 s68, s72, 32768\n"
        "s_add_u32 m0, s67, 8448\n"
        "s_nop 0\n"
        "buffer_load_dwordx4 v114, s[44:47], s68 offen  lds\n"
        "s_add_u32 m0, s67, 8992\n"
        "s_nop 0\n"
        "buffer_load_dwordx4 v114, s[44:47], s68 offen offset:512  lds\n"
        "s_add_u32 m0, s67, 9536\n"
        "s_nop 0\n"
        "buffer_load_dwordx4 v114, s[44:47], s68 offen offset:1024  lds\n"
        "s_add_u32 m0, s67, 10080\n"
        "s_nop 0\n"
        "buffer_load_dwordx4 v114, s[44:47], s68 offen offset:1536  lds\n"
        "v_mfma_f32_16x16x32_bf16 v[20:23], a[160:163], v[176:179], 0\n"
        "v_mfma_f32_16x16x32_bf16 v[20:23], a[164:167], v[180:183], v[20:23]\n"
        "v_mfma_f32_16x16x32_bf16 v[20:23], a[168:171], v[184:187], v[20:23]\n"
        "v_mfma_f32_16x16x32_bf16 v[20:23], a[172:175], v[188:191], v[20:23]\n"
        "v_max3_f32 v118, v118, v16, v17\n"
        "v_max3_f32 v118, v118, v18, v19\n"
        "v_max3_f32 v119, v119, v40, v41\n"
        "v_max3_f32 v119, v119, v42, v43\n"
        "v_mfma_f32_16x16x32_bf16 v[20:23], a[176:179], v[192:195], v[20:23]\n"
        "v_mfma_f32_16x16x32_bf16 v[20:23], a[180:183], v[196:199], v[20:23]\n"
        "v_mfma_f32_16x16x32_bf16 v[20:23], a[184:187], v[200:203], v[20:23]\n"
        "v_mfma_f32_16x16x32_bf16 v[20:23], a[188:191], v[204:207], v[20:23]\n"
        "v_mfma_f32_16x16x32_bf16 v[44:47], a[160:163], v[208:211], 0\n"
        "v_mfma_f32_16x16x32_bf16 v[44:47], a[164:167], v[212:215], v[44:47]\n"
        "v_mfma_f32_16x16x32_bf16 v[44:47], a[168:171], v[216:219], v[44:47]\n"
        "v_mfma_f32_16x16x32_bf16 v[44:47], a[172:175], v[220:223], v[44:47]\n"
        "v_mfma_f32_16x16x32_bf16 v[44:47], a[176:179], v[224:227], v[44:47]\n"
        "v_mfma_f32_16x16x32_bf16 v[44:47], a[180:183], v[228:231], v[44:47]\n"
        "v_mfma_f32_16x16x32_bf16 v[44:47], a[184:187], v[232:235], v[44:47]\n"
        "v_mfma_f32_16x16x32_bf16 v[44:47], a[188:191], v[236:239], v[44:47]\n"
        "v_add_u32 v108, 16640, v108\n"
        "v_xor_b32 v109, 0x8000, v109\n"
        "v_xor_b32 v112, 0x8000, v112\n"
        "v_xor_b32 v113, 0x8000, v113\n"
        "s_add_u32 s61, s61, 16640\n"
        "v_max3_f32 v118, v118, v20, v21\n"
        "v_max3_f32 v118, v118, v22, v23\n"
        "s_nop 2\n"
        "v_max3_f32 v119, v119, v44, v45\n"
        "v_max3_f32 v119, v119, v46, v47\n"
        "s_nop 1\n"
        "v_permlane32_swap_b32 v118, v119\n"
        "v_max_f32 v118, v118, v119\n"
        "v_mov_b32 v119, v118\n"
        "s_nop 1\n"
        "v_permlane16_swap_b32 v118, v119\n"
        "v_max_f32 v118, v118, v119\n"
        "ds_write_b32 v111, v118 offset:0\n"
        "s_nop 0\n"
        "v_xor_b32 v111, 0x8000, v111\n"
        "s_waitcnt vmcnt(12) lgkmcnt(0)\n"
        "s_barrier\n"
        "ds_read_b128 v[80:83], v112 offset:0\n"
        "v_add_u32 v110, s57, v109\n"
        "s_waitcnt vmcnt(8)\n"
        "ds_read_b64_tr_b16 a[192:193], v115 offset:0\n"
        "ds_read_b64_tr_b16 a[194:195], v115 offset:512\n"
        "ds_read_b64_tr_b16 a[196:197], v115 offset:32\n"
        "ds_read_b64_tr_b16 a[198:199], v115 offset:544\n"
        "ds_read_b64_tr_b16 a[200:201], v115 offset:64\n"
        "ds_read_b64_tr_b16 a[202:203], v115 offset:576\n"
        "ds_read_b64_tr_b16 a[204:205], v115 offset:96\n"
        "ds_read_b64_tr_b16 a[206:207], v115 offset:608\n"
        "s_waitcnt lgkmcnt(8)\n"
        "v_max3_f32 v118, v80, v81, v82\n"
        "v_max_f32 v118, v118, v83\n"
        "v_mul_f32 v118, -s52, v118\n"
        "v_fma_f32 v0, v0, s52, v118\n"
        "s_waitcnt lgkmcnt(0)\n"
        "v_exp_f32 v0, v0\n"
        "v_fma_f32 v1, v1, s52, v118\n"
        "v_fma_f32 v2, v2, s52, v118\n"
        "s_add_u32 s68, s73, 0\n"
        "v_exp_f32 v1, v1\n"
        "v_exp_f32 v2, v2\n"
        "s_add_u32 m0, s67, 0\n"
        "v_cvt_pk_bf16_f32 v0, v0, v1\n"
        "v_fma_f32 v3, v3, s52, v118\n"
        "v_exp_f32 v3, v3\n"
        "buffer_load_dwordx4 v114, s[44:47], s68 offen  lds\n"
        "v_cvt_pk_bf16_f32 v1, v2, v3\n"
        "v_fma_f32 v4, v4, s52, v118\n"
        "s_add_u32 m0, s67, 544\n"
        "ds_read_b128 v[80:83], v112 offset:256\n"
        "v_exp_f32 v4, v4\n"
        "v_fma_f32 v5, v5, s52, v118\n"
        "buffer_load_dwordx4 v114, s[44:47], s68 offen offset:512  lds\n"
        "v_exp_f32 v5, v5\n"
        "s_add_u32 m0, s67, 1088\n"
        "v_cvt_pk_bf16_f32 v2, v4, v5\n"
        "v_fma_f32 v6, v6, s52, v118\n"
        "v_exp_f32 v6, v6\n"
        "buffer_load_dwordx4 v114, s[44:47], s68 offen offset:1024  lds\n"
        "v_fma_f32 v7, v7, s52, v118\n"
        "v_fma_f32 v8, v8, s52, v118\n"
        "s_add_u32 m0, s67, 1632\n"
        "v_exp_f32 v7, v7\n"
        "v_exp_f32 v8, v8\n"
        "buffer_load_dwordx4 v114, s[44:47], s68 offen offset:1536  lds\n"
        "v_cvt_pk_bf16_f32 v3, v6, v7\n"
        "v_fma_f32 v9, v9, s52, v118\n"
        "s_waitcnt lgkmcnt(0)\n"
        "ds_write_b128 v109, v[0:3] offset:0\n"
        "v_exp_f32 v9, v9\n"
        "v_fma_f32 v10, v10, s52, v118\n"
        "v_fma_f32 v11, v11, s52, v118\n"
        "v_cvt_pk_bf16_f32 v8, v8, v9\n"
        "v_exp_f32 v10, v10\n"
        "v_fma_f32 v12, v12, s52, v118\n"
        "v_max3_f32 v119, v80, v81, v82\n"
        "v_max_f32 v119, v119, v83\n"
        "v_mul_f32 v119, -s52, v119\n"
        "v_fma_f32 v13, v13, s52, v118\n"
        "v_exp_f32 v11, v11\n"
        "v_exp_f32 v12, v12\n"
        "v_cvt_pk_bf16_f32 v9, v10, v11\n"
        "v_exp_f32 v13, v13\n"
        "v_fma_f32 v14, v14, s52, v118\n"
        "v_cvt_pk_bf16_f32 v10, v12, v13\n"
        "v_exp_f32 v14, v14\n"
        "v_fma_f32 v15, v15, s52, v118\n"
        "v_exp_f32 v15, v15\n"
        "v_fma_f32 v16, v16, s52, v118\n"
        "v_fma_f32 v17, v17, s52, v118\n"
        "v_cvt_pk_bf16_f32 v11, v14, v15\n"
        "v_exp_f32 v16, v16\n"
        "v_fma_f32 v18, v18, s52, v118\n"
        "ds_write_b128 v109, v[8:11] offset:1024\n"
        "v_exp_f32 v17, v17\n"
        "v_exp_f32 v18, v18\n"
        "v_cvt_pk_bf16_f32 v16, v16, v17\n"
        "v_fma_f32 v19, v19, s52, v118\n"
        "v_exp_f32 v19, v19\n"
        "ds_read_b128 v[84:87], v108 offset:0\n"
        "v_cvt_pk_bf16_f32 v17, v18, v19\n"
        "v_fma_f32 v20, v20, s52, v118\n"
        "v_fma_f32 v24, v24, s52, v119\n"
        "ds_read_b128 v[88:91], v108 offset:32\n"
        "v_exp_f32 v20, v20\n"
        "v_fma_f32 v21, v21, s52, v118\n"
        "ds_read_b128 v[92:95], v108 offset:64\n"
        "v_exp_f32 v24, v24\n"
        "v_fma_f32 v25, v25, s52, v119\n"
        "ds_read_b128 v[96:99], v108 offset:96\n"
        "v_exp_f32 v21, v21\n"
        "v_fma_f32 v22, v22, s52, v118\n"
        "v_exp_f32 v22, v22\n"
        "v_fma_f32 v23, v23, s52, v118\n"
        "v_exp_f32 v23, v23\n"
        "v_cvt_pk_bf16_f32 v18, v20, v21\n"
        "v_cvt_pk_bf16_f32 v19, v22, v23\n"
        "v_exp_f32 v25, v25\n"
        "s_nop 0\n"
        "v_cvt_pk_bf16_f32 v24, v24, v25\n"
        "v_fma_f32 v26, v26, s52, v119\n"
        "v_exp_f32 v26, v26\n"
        "v_mfma_f32_16x16x32_bf16 v[80:83], v[124:127], v[0:3], 0\n"
        "v_fma_f32 v27, v27, s52, v119\n"
        "v_fma_f32 v28, v28, s52, v119\n"
        "v_mfma_f32_16x16x32_bf16 v[80:83], v[124:127], v[8:11], v[80:83]\n"
        "v_exp_f32 v27, v27\n"
        "v_mfma_f32_16x16x32_bf16 v[80:83], v[124:127], v[16:19], v[80:83]\n"
        "s_waitcnt lgkmcnt(0)\n"
        "v_cvt_pk_bf16_f32 v25, v26, v27\n"
        "v_fma_f32 v29, v29, s52, v119\n"
        "v_mfma_f32_16x16x32_bf16 v[0:3], a[0:3], v[84:87], 0\n"
        "v_exp_f32 v28, v28\n"
        "v_mfma_f32_16x16x32_bf16 v[0:3], a[4:7], v[88:91], v[0:3]\n"
        "s_waitcnt vmcnt(8)\n"
        "ds_read_b64_tr_b16 a[208:209], v115 offset:4224\n"
        "v_mfma_f32_16x16x32_bf16 v[0:3], a[8:11], v[92:95], v[0:3]\n"
        "ds_read_b64_tr_b16 a[210:211], v115 offset:4736\n"
        "ds_read_b64_tr_b16 a[212:213], v115 offset:4256\n"
        "v_mfma_f32_16x16x32_bf16 v[0:3], a[12:15], v[96:99], v[0:3]\n"
        "ds_write_b32 v113, v80 offset:512\n"
        "v_mfma_f32_16x16x32_bf16 v[4:7], a[32:35], v[84:87], 0\n"
        "ds_read_b64_tr_b16 a[214:215], v115 offset:4768\n"
        "ds_read_b64_tr_b16 a[216:217], v115 offset:4288\n"
        "v_mfma_f32_16x16x32_bf16 v[4:7], a[36:39], v[88:91], v[4:7]\n"
        "ds_read_b64_tr_b16 a[218:219], v115 offset:4800\n"
        "ds_read_b64_tr_b16 a[220:221], v115 offset:4320\n"
        "v_mfma_f32_16x16x32_bf16 v[4:7], a[40:43], v[92:95], v[4:7]\n"
        "ds_read_b64_tr_b16 a[222:223], v115 offset:4832\n"
        "v_fma_f32 v30, v30, s52, v119\n"
        "v_mfma_f32_16x16x32_bf16 v[4:7], a[44:47], v[96:99], v[4:7]\n"
        "v_exp_f32 v29, v29\n"
        "v_mfma_f32_16x16x32_bf16 v[8:11], a[64:67], v[84:87], 0\n"
        "v_cvt_pk_bf16_f32 v26, v28, v29\n"
        "v_fma_f32 v31, v31, s52, v119\n"
        "v_mfma_f32_16x16x32_bf16 v[8:11], a[68:71], v[88:91], v[8:11]\n"
        "v_exp_f32 v30, v30\n"
        "v_mfma_f32_16x16x32_bf16 v[8:11], a[72:75], v[92:95], v[8:11]\n"
        "s_waitcnt lgkmcnt(0)\n"
        "v_exp_f32 v31, v31\n"
        "v_mfma_f32_16x16x32_bf16 v[8:11], a[76:79], v[96:99], v[8:11]\n"
        "s_add_u32 s68, s73, 16384\n"
        "v_cvt_pk_bf16_f32 v27, v30, v31\n"
        "v_fma_f32 v32, v32, s52, v119\n"
        "v_mfma_f32_16x16x32_bf16 v[12:15], a[96:99], v[84:87], 0\n"
        "s_add_u32 m0, s67, 4224\n"
        "v_exp_f32 v32, v32\n"
        "v_mfma_f32_16x16x32_bf16 v[12:15], a[100:103], v[88:91], v[12:15]\n"
        "buffer_load_dwordx4 v114, s[44:47], s68 offen  lds\n"
        "v_mfma_f32_16x16x32_bf16 v[12:15], a[104:107], v[92:95], v[12:15]\n"
        "s_add_u32 m0, s67, 4768\n"
        "ds_read_b128 v[80:83], v108 offset:128\n"
        "v_fma_f32 v33, v33, s52, v119\n"
        "v_mfma_f32_16x16x32_bf16 v[12:15], a[108:111], v[96:99], v[12:15]\n"
        "ds_read_b128 v[100:103], v108 offset:160\n"
        "ds_write_b128 v109, v[16:19] offset:2048\n"
        "v_mfma_f32_16x16x32_bf16 v[16:19], a[128:131], v[84:87], 0\n"
        "ds_read_b128 v[104:107], v108 offset:192\n"
        "v_fma_f32 v34, v34, s52, v119\n"
        "v_mfma_f32_16x16x32_bf16 v[16:19], a[132:135], v[88:91], v[16:19]\n"
        "buffer_load_dwordx4 v114, s[44:47], s68 offen offset:512  lds\n"
        "v_mfma_f32_16x16x32_bf16 v[16:19], a[136:139], v[92:95], v[16:19]\n"
        "s_add_u32 m0, s67, 5312\n"
        "v_exp_f32 v33, v33\n"
        "v_mfma_f32_16x16x32_bf16 v[16:19], a[140:143], v[96:99], v[16:19]\n"
        "buffer_load_dwordx4 v114, s[44:47], s68 offen offset:1024  lds\n"
        "v_mfma_f32_16x16x32_bf16 v[20:23], a[160:163], v[84:87], 0\n"
        "s_add_u32 m0, s67, 5856\n"
        "ds_read_b128 v[84:87], v108 offset:224\n"
        "v_cvt_pk_bf16_f32 v32, v32, v33\n"
        "v_mfma_f32_16x16x32_bf16 v[20:23], a[164:167], v[88:91], v[20:23]\n"
        "s_mov_b32 s64, s61\n"
        "buffer_load_dwordx4 v114, s[44:47], s68 offen offset:1536  lds\n"
        "v_mfma_f32_16x16x32_bf16 v[20:23], a[168:171], v[92:95], v[20:23]\n"
        "s_add_u32 s61, s61, 16640\n"
        "ds_write_b128 v109, v[24:27] offset:3072\n"
        "v_mfma_f32_16x16x32_bf16 v[20:23], a[172:175], v[96:99], v[20:23]\n"
        "s_waitcnt lgkmcnt(2)\n"
        "v_exp_f32 v34, v34\n"
        "v_mfma_f32_16x16x32_bf16 v[0:3], a[16:19], v[80:83], v[0:3]\n"
        "v_fma_f32 v35, v35, s52, v119\n"
        "v_fma_f32 v36, v36, s52, v119\n"
        "v_mfma_f32_16x16x32_bf16 v[0:3], a[20:23], v[100:103], v[0:3]\n"
        "s_waitcnt lgkmcnt(1)\n"
        "s_waitcnt vmcnt(8)\n"
        "ds_read_b64_tr_b16 a[224:225], v115 offset:8448\n"
        "v_mfma_f32_16x16x32_bf16 v[0:3], a[24:27], v[104:107], v[0:3]\n"
        "ds_read_b64_tr_b16 a[226:227], v115 offset:8960\n"
        "ds_read_b64_tr_b16 a[228:229], v115 offset:8480\n"
        "v_mfma_f32_16x16x32_bf16 v[0:3], a[28:31], v[84:87], v[0:3]\n"
        "s_cmp_eq_u32 s61, 108288\n"
        "s_cselect_b32 s61, 58368, s61\n"
        "ds_read_b64_tr_b16 a[230:231], v115 offset:8992\n"
        "ds_read_b64_tr_b16 a[232:233], v115 offset:8512\n"
        "v_mfma_f32_16x16x32_bf16 v[4:7], a[48:51], v[80:83], v[4:7]\n"
        "s_sub_u32 s62, s61, s64\n"
        "ds_read_b64_tr_b16 a[234:235], v115 offset:9024\n"
        "ds_read_b64_tr_b16 a[236:237], v115 offset:8544\n"
        "v_mfma_f32_16x16x32_bf16 v[4:7], a[52:55], v[100:103], v[4:7]\n"
        "ds_read_b64_tr_b16 a[238:239], v115 offset:9056\n"
        "v_fma_f32 v37, v37, s52, v119\n"
        "v_mfma_f32_16x16x32_bf16 v[4:7], a[56:59], v[104:107], v[4:7]\n"
        "v_exp_f32 v35, v35\n"
        "v_mfma_f32_16x16x32_bf16 v[4:7], a[60:63], v[84:87], v[4:7]\n"
        "s_mov_b32 s55, s64\n"
        "v_cvt_pk_bf16_f32 v33, v34, v35\n"
        "v_fma_f32 v38, v38, s52, v119\n"
        "v_mfma_f32_16x16x32_bf16 v[8:11], a[80:83], v[80:83], v[8:11]\n"
        "v_exp_f32 v36, v36\n"
        "v_mfma_f32_16x16x32_bf16 v[8:11], a[84:87], v[100:103], v[8:11]\n"
        "s_waitcnt lgkmcnt(0)\n"
        "v_exp_f32 v37, v37\n"
        "v_mfma_f32_16x16x32_bf16 v[8:11], a[88:91], v[104:107], v[8:11]\n"
        "s_add_u32 s68, s73, 32768\n"
        "v_cvt_pk_bf16_f32 v34, v36, v37\n"
        "v_fma_f32 v39, v39, s52, v119\n"
        "v_mfma_f32_16x16x32_bf16 v[8:11], a[92:95], v[84:87], v[8:11]\n"
        "s_add_u32 m0, s67, 8448\n"
        "v_exp_f32 v38, v38\n"
        "v_mfma_f32_16x16x32_bf16 v[12:15], a[112:115], v[80:83], v[12:15]\n"
        "buffer_load_dwordx4 v114, s[44:47], s68 offen  lds\n"
        "v_mfma_f32_16x16x32_bf16 v[12:15], a[116:119], v[100:103], v[12:15]\n"
        "s_add_u32 m0, s67, 8992\n"
        "v_exp_f32 v39, v39\n"
        "v_mfma_f32_16x16x32_bf16 v[12:15], a[120:123], v[104:107], v[12:15]\n"
        "buffer_load_dwordx4 v114, s[44:47], s68 offen offset:512  lds\n"
        "v_mfma_f32_16x16x32_bf16 v[12:15], a[124:127], v[84:87], v[12:15]\n"
        "s_add_u32 m0, s67, 9536\n"
        "v_cvt_pk_bf16_f32 v35, v38, v39\n"
        "v_fma_f32 v40, v40, s52, v119\n"
        "v_mfma_f32_16x16x32_bf16 v[16:19], a[144:147], v[80:83], v[16:19]\n"
        "buffer_load_dwordx4 v114, s[44:47], s68 offen offset:1024  lds\n"
        "v_mfma_f32_16x16x32_bf16 v[16:19], a[148:151], v[100:103], v[16:19]\n"
        "s_add_u32 m0, s67, 10080\n"
        "ds_read_b128 v[96:99], v108 offset:512\n"
        "v_fma_f32 v41, v41, s52, v119\n"
        "v_mfma_f32_16x16x32_bf16 v[16:19], a[152:155], v[104:107], v[16:19]\n"
        "ds_read_b128 v[92:95], v108 offset:544\n"
        "v_fma_f32 v42, v42, s52, v119\n"
        "v_mfma_f32_16x16x32_bf16 v[16:19], a[156:159], v[84:87], v[16:19]\n"
        "buffer_load_dwordx4 v114, s[44:47], s68 offen offset:1536  lds\n"
        "v_mfma_f32_16x16x32_bf16 v[20:23], a[176:179], v[80:83], v[20:23]\n"
        "s_add_u32 m0, s69, 91648\n"
        "v_exp_f32 v40, v40\n"
        "v_mfma_f32_16x16x32_bf16 v[20:23], a[180:183], v[100:103], v[20:23]\n"
        "ds_read_b128 v[100:103], v108 offset:576\n"
        "v_exp_f32 v41, v41\n"
        "v_exp_f32 v42, v42\n"
        "v_fma_f32 v43, v43, s52, v119\n"
        "v_exp_f32 v43, v43\n"
        "v_fma_f32 v44, v44, s52, v119\n"
        "v_exp_f32 v44, v44\n"
        "v_fma_f32 v45, v45, s52, v119\n"
        "v_exp_f32 v45, v45\n"
        "v_fma_f32 v46, v46, s52, v119\n"
        "v_exp_f32 v46, v46\n"
        "v_fma_f32 v47, v47, s52, v119\n"
        "v_exp_f32 v47, v47\n"
        "v_mfma_f32_16x16x32_bf16 v[20:23], a[184:187], v[104:107], v[20:23]\n"
        "ds_read_b128 v[104:107], v108 offset:608\n"
        "v_cvt_pk_bf16_f32 v40, v40, v41\n"
        "v_cvt_pk_bf16_f32 v41, v42, v43\n"
        "v_cvt_pk_bf16_f32 v42, v44, v45\n"
        "v_cvt_pk_bf16_f32 v43, v46, v47\n"
        "v_mfma_f32_16x16x32_bf16 v[20:23], a[188:191], v[84:87], v[20:23]\n"
        "buffer_load_dwordx4 v117, s[36:39], s54 offen nt lds\n"
        "v_mfma_f32_16x16x32_bf16 v[84:87], v[124:127], v[24:27], 0\n"
        "s_add_u32 m0, s69, 92176\n"
        "ds_write_b128 v109, v[32:35] offset:4096\n"
        "v_mfma_f32_16x16x32_bf16 v[84:87], v[124:127], v[32:35], v[84:87]\n"
        "buffer_load_dwordx4 v117, s[36:39], s54 offen offset:512 nt lds\n"
        "v_mfma_f32_16x16x32_bf16 v[84:87], v[124:127], v[40:43], v[84:87]\n"
        "s_waitcnt lgkmcnt(2)\n"
        "v_max_f32 v118, v0, v1\n"
        "v_max3_f32 v118, v118, v2, v3\n"
        "v_mfma_f32_16x16x32_bf16 v[24:27], a[0:3], v[96:99], 0\n"
        "s_waitcnt lgkmcnt(1)\n"
        "s_add_u32 m0, s69, 92704\n"
        "ds_write_b128 v109, v[40:43] offset:5120\n"
        "v_mfma_f32_16x16x32_bf16 v[24:27], a[4:7], v[92:95], v[24:27]\n"
        "buffer_load_dwordx4 v117, s[36:39], s54 offen offset:1024 nt lds\n"
        "v_mfma_f32_16x16x32_bf16 v[24:27], a[8:11], v[100:103], v[24:27]\n"
        "s_add_u32 m0, s69, 93232\n"
        "v_max3_f32 v118, v118, v4, v5\n"
        "v_mfma_f32_16x16x32_bf16 v[24:27], a[12:15], v[104:107], v[24:27]\n"
        "buffer_load_dwordx4 v117, s[36:39], s54 offen offset:1536 nt lds\n"
        "ds_write_b32 v113, v84 offset:768\n"
        "v_mfma_f32_16x16x32_bf16 v[28:31], a[32:35], v[96:99], 0\n"
        "s_add_u32 s54, s54, 16384\n"
        "v_max3_f32 v118, v118, v6, v7\n"
        "v_max3_f32 v118, v118, v8, v9\n"
        "v_mfma_f32_16x16x32_bf16 v[28:31], a[36:39], v[92:95], v[28:31]\n"
        "v_max3_f32 v118, v118, v10, v11\n"
        "v_max3_f32 v118, v118, v12, v13\n"
        "v_mfma_f32_16x16x32_bf16 v[28:31], a[40:43], v[100:103], v[28:31]\n"
        "v_max3_f32 v118, v118, v14, v15\n"
        "v_mfma_f32_16x16x32_bf16 v[28:31], a[44:47], v[104:107], v[28:31]\n"
        "v_max3_f32 v118, v118, v16, v17\n"
        "v_max3_f32 v118, v118, v18, v19\n"
        "v_mfma_f32_16x16x32_bf16 v[32:35], a[64:67], v[96:99], 0\n"
        "s_waitcnt vmcnt(12)\n"
        "ds_read_b64_tr_b16 a[240:241], v115 offset:0\n"
        "ds_read_b64_tr_b16 a[242:243], v115 offset:512\n"
        "v_mfma_f32_16x16x32_bf16 v[32:35], a[68:71], v[92:95], v[32:35]\n"
        "ds_read_b64_tr_b16 a[244:245], v115 offset:32\n"
        "ds_read_b64_tr_b16 a[246:247], v115 offset:544\n"
        "v_mfma_f32_16x16x32_bf16 v[32:35], a[72:75], v[100:103], v[32:35]\n"
        "ds_read_b64_tr_b16 a[248:249], v115 offset:64\n"
        "ds_read_b64_tr_b16 a[250:251], v115 offset:576\n"
        "v_mfma_f32_16x16x32_bf16 v[32:35], a[76:79], v[104:107], v[32:35]\n"
        "ds_read_b64_tr_b16 a[252:253], v115 offset:96\n"
        "ds_read_b64_tr_b16 a[254:255], v115 offset:608\n"
        "v_mfma_f32_16x16x32_bf16 v[36:39], a[96:99], v[96:99], 0\n"
        "v_max3_f32 v118, v118, v20, v21\n"
        "v_max3_f32 v118, v118, v22, v23\n"
        "v_mfma_f32_16x16x32_bf16 v[36:39], a[100:103], v[92:95], v[36:39]\n"
        "v_xor_b32 v112, 0x8000, v112\n"
        "v_xor_b32 v113, 0x8000, v113\n"
        "v_mfma_f32_16x16x32_bf16 v[36:39], a[104:107], v[100:103], v[36:39]\n"
        "ds_read_b128 v[84:87], v108 offset:640\n"
        "v_mfma_f32_16x16x32_bf16 v[36:39], a[108:111], v[104:107], v[36:39]\n"
        "s_waitcnt lgkmcnt(1)\n"
        "ds_read_b128 v[80:83], v108 offset:672\n"
        "v_mfma_f32_16x16x32_bf16 v[40:43], a[128:131], v[96:99], 0\n"
        "s_add_u32 s68, s74, 0\n"
        "ds_read_b128 v[88:91], v108 offset:704\n"
        "v_mfma_f32_16x16x32_bf16 v[40:43], a[132:135], v[92:95], v[40:43]\n"
        "s_add_u32 m0, s67, 0\n"
        "v_mfma_f32_16x16x32_bf16 v[40:43], a[136:139], v[100:103], v[40:43]\n"
        "buffer_load_dwordx4 v114, s[44:47], s68 offen  lds\n"
        "v_mfma_f32_16x16x32_bf16 v[40:43], a[140:143], v[104:107], v[40:43]\n"
        "s_add_u32 m0, s67, 544\n"
        "v_mfma_f32_16x16x32_bf16 v[44:47], a[160:163], v[96:99], 0\n"
        "ds_read_b128 v[96:99], v108 offset:736\n"
        "v_add_u32 v108, s62, v108\n"
        "v_mfma_f32_16x16x32_bf16 v[44:47], a[164:167], v[92:95], v[44:47]\n"
        "buffer_load_dwordx4 v114, s[44:47], s68 offen offset:512  lds\n"
        "v_mfma_f32_16x16x32_bf16 v[44:47], a[168:171], v[100:103], v[44:47]\n"
        "s_add_u32 m0, s67, 1088\n"
        "v_mfma_f32_16x16x32_bf16 v[44:47], a[172:175], v[104:107], v[44:47]\n"
        "s_waitcnt lgkmcnt(1)\n"
        "buffer_load_dwordx4 v114, s[44:47], s68 offen offset:1024  lds\n"
        "v_mfma_f32_16x16x32_bf16 v[24:27], a[16:19], v[84:87], v[24:27]\n"
        "v_mfma_f32_16x16x32_bf16 v[24:27], a[20:23], v[80:83], v[24:27]\n"
        "s_waitcnt lgkmcnt(0)\n"
        "v_mfma_f32_16x16x32_bf16 v[24:27], a[24:27], v[88:91], v[24:27]\n"
        "v_mfma_f32_16x16x32_bf16 v[24:27], a[28:31], v[96:99], v[24:27]\n"
        "s_add_u32 m0, s67, 1632\n"
        "v_mfma_f32_16x16x32_bf16 v[28:31], a[48:51], v[84:87], v[28:31]\n"
        "buffer_load_dwordx4 v114, s[44:47], s68 offen offset:1536  lds\n"
        "v_mfma_f32_16x16x32_bf16 v[28:31], a[52:55], v[80:83], v[28:31]\n"
        "v_mfma_f32_16x16x32_bf16 v[28:31], a[56:59], v[88:91], v[28:31]\n"
        "v_max_f32 v119, v24, v25\n"
        "v_max3_f32 v119, v119, v26, v27\n"
        "v_mfma_f32_16x16x32_bf16 v[28:31], a[60:63], v[96:99], v[28:31]\n"
        "v_mfma_f32_16x16x32_bf16 v[32:35], a[80:83], v[84:87], v[32:35]\n"
        "v_mfma_f32_16x16x32_bf16 v[32:35], a[84:87], v[80:83], v[32:35]\n"
        "v_mfma_f32_16x16x32_bf16 v[32:35], a[88:91], v[88:91], v[32:35]\n"
        "s_waitcnt vmcnt(12)\n"
        "ds_read_b64_tr_b16 v[128:129], v115 offset:4224\n"
        "ds_read_b64_tr_b16 v[130:131], v115 offset:4736\n"
        "v_mfma_f32_16x16x32_bf16 v[32:35], a[92:95], v[96:99], v[32:35]\n"
        "ds_read_b64_tr_b16 v[132:133], v115 offset:4256\n"
        "ds_read_b64_tr_b16 v[134:135], v115 offset:4768\n"
        "v_mfma_f32_16x16x32_bf16 v[36:39], a[112:115], v[84:87], v[36:39]\n"
        "ds_read_b64_tr_b16 v[136:137], v115 offset:4288\n"
        "ds_read_b64_tr_b16 v[138:139], v115 offset:4800\n"
        "v_mfma_f32_16x16x32_bf16 v[36:39], a[116:119], v[80:83], v[36:39]\n"
        "ds_read_b64_tr_b16 v[140:141], v115 offset:4320\n"
        "ds_read_b64_tr_b16 v[142:143], v115 offset:4832\n"
        "v_mfma_f32_16x16x32_bf16 v[36:39], a[120:123], v[88:91], v[36:39]\n"
        "ds_read_b128 v[104:107], v109 offset:0\n"
        "v_max3_f32 v119, v119, v28, v29\n"
        "v_mfma_f32_16x16x32_bf16 v[36:39], a[124:127], v[96:99], v[36:39]\n"
        "ds_read_b128 v[100:103], v109 offset:1024\n"
        "v_max3_f32 v119, v119, v30, v31\n"
        "v_mfma_f32_16x16x32_bf16 v[40:43], a[144:147], v[84:87], v[40:43]\n"
        "ds_read_b128 v[92:95], v109 offset:2048\n"
        "v_max3_f32 v119, v119, v32, v33\n"
        "v_mfma_f32_16x16x32_bf16 v[40:43], a[148:151], v[80:83], v[40:43]\n"
        "s_waitcnt lgkmcnt(3)\n"
        "v_max3_f32 v119, v119, v34, v35\n"
        "v_mfma_f32_16x16x32_bf16 v[40:43], a[152:155], v[88:91], v[40:43]\n"
        "s_add_u32 s68, s74, 16384\n"
        "v_max3_f32 v119, v119, v36, v37\n"
        "v_max3_f32 v119, v119, v38, v39\n"
        "v_mfma_f32_16x16x32_bf16 v[40:43], a[156:159], v[96:99], v[40:43]\n"
        "s_add_u32 m0, s67, 4224\n"
        "v_mfma_f32_16x16x32_bf16 v[44:47], a[176:179], v[84:87], v[44:47]\n"
        "ds_read_b128 v[84:87], v109 offset:4096\n"
        "v_mfma_f32_16x16x32_bf16 v[44:47], a[180:183], v[80:83], v[44:47]\n"
        "ds_read_b128 v[80:83], v109 offset:3072\n"
        "v_mfma_f32_16x16x32_bf16 v[44:47], a[184:187], v[88:91], v[44:47]\n"
        "ds_read_b128 v[88:91], v109 offset:5120\n"
        "v_max3_f32 v119, v119, v40, v41\n"
        "v_mfma_f32_16x16x32_bf16 v[44:47], a[188:191], v[96:99], v[44:47]\n"
        "s_waitcnt lgkmcnt(3)\n"
        "buffer_load_dwordx4 v114, s[44:47], s68 offen  lds\n"
        "v_mfma_f32_16x16x32_bf16 v[48:51], a[192:195], v[104:107], 0\n"
        "v_max3_f32 v119, v119, v42, v43\n"
        "v_xor_b32 v109, 0x8000, v109\n"
        "v_mfma_f32_16x16x32_bf16 v[48:51], a[208:211], v[100:103], v[48:51]\n"
        "s_add_u32 m0, s67, 4768\n"
        "v_mfma_f32_16x16x32_bf16 v[48:51], a[224:227], v[92:95], v[48:51]\n"
        "buffer_load_dwordx4 v114, s[44:47], s68 offen offset:512  lds\n"
        "v_mfma_f32_16x16x32_bf16 v[52:55], a[196:199], v[104:107], 0\n"
        "s_add_u32 m0, s67, 5312\n"
        "v_max3_f32 v119, v119, v44, v45\n"
        "v_max3_f32 v119, v119, v46, v47\n"
        "v_mfma_f32_16x16x32_bf16 v[52:55], a[212:215], v[100:103], v[52:55]\n"
        "buffer_load_dwordx4 v114, s[44:47], s68 offen offset:1024  lds\n"
        "v_mfma_f32_16x16x32_bf16 v[52:55], a[228:231], v[92:95], v[52:55]\n"
        "s_add_u32 m0, s67, 5856\n"
        "v_permlane32_swap_b32 v118, v119\n"
        "v_max_f32 v118, v118, v119\n"
        "v_mov_b32 v119, v118\n"
        "v_mfma_f32_16x16x32_bf16 v[56:59], a[200:203], v[104:107], 0\n"
        "buffer_load_dwordx4 v114, s[44:47], s68 offen offset:1536  lds\n"
        "v_permlane16_swap_b32 v118, v119\n"
        "v_max_f32 v118, v118, v119\n"
        "v_mfma_f32_16x16x32_bf16 v[56:59], a[216:219], v[100:103], v[56:59]\n"
        "ds_write_b32 v111, v118 offset:0\n"
        "v_mfma_f32_16x16x32_bf16 v[56:59], a[232:235], v[92:95], v[56:59]\n"
        "v_xor_b32 v111, 0x8000, v111\n"
        "v_mfma_f32_16x16x32_bf16 v[60:63], a[204:207], v[104:107], 0\n"
        "v_mfma_f32_16x16x32_bf16 v[60:63], a[220:223], v[100:103], v[60:63]\n"
        "v_mfma_f32_16x16x32_bf16 v[60:63], a[236:239], v[92:95], v[60:63]\n"
        "s_waitcnt vmcnt(8) lgkmcnt(0)\n"
        "s_barrier\n"
        "v_mfma_f32_16x16x32_bf16 v[64:67], a[192:195], v[80:83], 0\n"
        "s_waitcnt vmcnt(12)\n"
        "ds_read_b128 v[92:95], v112 offset:0\n"
        "ds_read_b64_tr_b16 v[144:145], v115 offset:8448\n"
        "v_mfma_f32_16x16x32_bf16 v[64:67], a[208:211], v[84:87], v[64:67]\n"
        "ds_read_b64_tr_b16 v[146:147], v115 offset:8960\n"
        "ds_read_b64_tr_b16 v[148:149], v115 offset:8480\n"
        "v_mfma_f32_16x16x32_bf16 v[64:67], a[224:227], v[88:91], v[64:67]\n"
        "ds_read_b64_tr_b16 v[150:151], v115 offset:8992\n"
        "ds_read_b64_tr_b16 v[152:153], v115 offset:8512\n"
        "v_mfma_f32_16x16x32_bf16 v[68:71], a[196:199], v[80:83], 0\n"
        "ds_read_b64_tr_b16 v[154:155], v115 offset:9024\n"
        "ds_read_b64_tr_b16 v[156:157], v115 offset:8544\n"
        "v_mfma_f32_16x16x32_bf16 v[68:71], a[212:215], v[84:87], v[68:71]\n"
        "ds_read_b64_tr_b16 v[158:159], v115 offset:9056\n"
        "v_mfma_f32_16x16x32_bf16 v[68:71], a[228:231], v[88:91], v[68:71]\n"
        "s_waitcnt lgkmcnt(8)\n"
        "v_mfma_f32_16x16x32_bf16 v[72:75], a[200:203], v[80:83], 0\n"
        "v_max3_f32 v118, v92, v93, v94\n"
        "v_max_f32 v118, v118, v95\n"
        "v_mfma_f32_16x16x32_bf16 v[72:75], a[216:219], v[84:87], v[72:75]\n"
        "v_mul_f32 v118, -s52, v118\n"
        "v_fma_f32 v0, v0, s52, v118\n"
        "v_mfma_f32_16x16x32_bf16 v[72:75], a[232:235], v[88:91], v[72:75]\n"
        "s_waitcnt lgkmcnt(0)\n"
        "v_exp_f32 v0, v0\n"
        "v_mfma_f32_16x16x32_bf16 v[76:79], a[204:207], v[80:83], 0\n"
        "s_add_u32 s68, s74, 32768\n"
        "v_fma_f32 v1, v1, s52, v118\n"
        "v_fma_f32 v2, v2, s52, v118\n"
        "v_mfma_f32_16x16x32_bf16 v[76:79], a[220:223], v[84:87], v[76:79]\n"
        "s_add_u32 m0, s67, 8448\n"
        "v_exp_f32 v1, v1\n"
        "v_mfma_f32_16x16x32_bf16 v[76:79], a[236:239], v[88:91], v[76:79]\n"
        "ds_read_b128 v[88:91], v112 offset:256\n"
        "v_cvt_pk_bf16_f32 v0, v0, v1\n"
        "buffer_load_dwordx4 v114, s[44:47], s68 offen  lds\n"
        "v_exp_f32 v2, v2\n"
        "s_add_u32 m0, s67, 8992\n"
        "v_fma_f32 v3, v3, s52, v118\n"
        "v_exp_f32 v3, v3\n"
        "v_fma_f32 v4, v4, s52, v118\n"
        "buffer_load_dwordx4 v114, s[44:47], s68 offen offset:512  lds\n"
        "v_cvt_pk_bf16_f32 v1, v2, v3\n"
        "v_fma_f32 v5, v5, s52, v118\n"
        "s_add_u32 m0, s67, 9536\n"
        "v_exp_f32 v4, v4\n"
        "v_exp_f32 v5, v5\n"
        "buffer_load_dwordx4 v114, s[44:47], s68 offen offset:1024  lds\n"
        "v_cvt_pk_bf16_f32 v2, v4, v5\n"
        "v_fma_f32 v6, v6, s52, v118\n"
        "s_add_u32 m0, s67, 10080\n"
        "v_exp_f32 v6, v6\n"
        "v_fma_f32 v7, v7, s52, v118\n"
        "v_fma_f32 v8, v8, s52, v118\n"
        "s_waitcnt lgkmcnt(0)\n"
        "buffer_load_dwordx4 v114, s[44:47], s68 offen offset:1536  lds\n"
        "v_exp_f32 v7, v7\n"
        "s_nop 0\n"
        "v_cvt_pk_bf16_f32 v3, v6, v7\n"
        "v_exp_f32 v8, v8\n"
        "v_fma_f32 v9, v9, s52, v118\n"
        "v_max3_f32 v119, v88, v89, v90\n"
        "v_max_f32 v119, v119, v91\n"
        "v_mul_f32 v119, -s52, v119\n"
        "v_fma_f32 v10, v10, s52, v118\n"
        "ds_write_b128 v109, v[0:3] offset:0\n"
        "v_exp_f32 v9, v9\n"
        "v_exp_f32 v10, v10\n"
        "ds_read_b128 v[88:91], v112 offset:512\n"
        "v_cvt_pk_bf16_f32 v8, v8, v9\n"
        "v_fma_f32 v11, v11, s52, v118\n"
        "v_fma_f32 v12, v12, s52, v118\n"
        "v_exp_f32 v11, v11\n"
        "v_exp_f32 v12, v12\n"
        "v_cvt_pk_bf16_f32 v9, v10, v11\n"
        "v_fma_f32 v13, v13, s52, v118\n"
        "v_exp_f32 v13, v13\n"
        "s_nop 0\n"
        "v_cvt_pk_bf16_f32 v10, v12, v13\n"
        "v_fma_f32 v14, v14, s52, v118\n"
        "v_exp_f32 v14, v14\n"
        "v_fma_f32 v15, v15, s52, v118\n"
        "v_exp_f32 v15, v15\n"
        "v_fma_f32 v16, v16, s52, v118\n"
        "v_cvt_pk_bf16_f32 v11, v14, v15\n"
        "v_exp_f32 v16, v16\n"
        "v_fma_f32 v17, v17, s52, v118\n"
        "s_waitcnt lgkmcnt(0)\n"
        "ds_write_b128 v109, v[8:11] offset:1024\n"
        "s_waitcnt vmcnt(8)\n"
        "ds_read_b64_tr_b16 v[160:161], v115 offset:0\n"
        "ds_read_b64_tr_b16 v[162:163], v115 offset:512\n"
        "ds_read_b64_tr_b16 v[164:165], v115 offset:32\n"
        "ds_read_b64_tr_b16 v[166:167], v115 offset:544\n"
        "ds_read_b128 v[84:87], v108 offset:0\n"
        "ds_read_b64_tr_b16 v[168:169], v115 offset:64\n"
        "ds_read_b64_tr_b16 v[170:171], v115 offset:576\n"
        "ds_read_b64_tr_b16 v[172:173], v115 offset:96\n"
        "ds_read_b128 v[80:83], v108 offset:32\n"
        "ds_read_b64_tr_b16 v[174:175], v115 offset:608\n"
        "v_exp_f32 v17, v17\n"
        "ds_read_b128 v[96:99], v108 offset:64\n"
        "v_add_f32 v120, v88, v89\n"
        "v_add_f32 v90, v90, v91\n"
        "v_add_f32 v120, v120, v90\n"
        "ds_read_b128 v[100:103], v108 offset:96\n"
        "v_cvt_pk_bf16_f32 v16, v16, v17\n"
        "v_fma_f32 v18, v18, s52, v118\n"
        "v_fma_f32 v19, v19, s52, v118\n"
        "ds_read_b128 v[92:95], v112 offset:768\n"
        "v_exp_f32 v18, v18\n"
        "v_exp_f32 v19, v19\n"
        "v_fma_f32 v20, v20, s52, v118\n"
        "v_exp_f32 v20, v20\n"
        "v_fma_f32 v21, v21, s52, v118\n"
        "v_exp_f32 v21, v21\n"
        "v_fma_f32 v22, v22, s52, v118\n"
        "v_exp_f32 v22, v22\n"
        "v_fma_f32 v23, v23, s52, v118\n"
        "v_exp_f32 v23, v23\n"
        "s_waitcnt lgkmcnt(3)\n"
        "v_cvt_pk_bf16_f32 v17, v18, v19\n"
        "v_cvt_pk_bf16_f32 v18, v20, v21\n"
        "v_cvt_pk_bf16_f32 v19, v22, v23\n"
        "v_fma_f32 v24, v24, s52, v119\n"
        "s_add_u32 s68, s75, 0\n"
        "v_exp_f32 v24, v24\n"
        "v_fma_f32 v25, v25, s52, v119\n"
        "v_fma_f32 v26, v26, s52, v119\n"
        "v_mfma_f32_16x16x32_bf16 v[88:91], v[124:127], v[0:3], 0\n"
        "s_add_u32 m0, s67, 0\n"
        "v_exp_f32 v25, v25\n"
        "v_mfma_f32_16x16x32_bf16 v[88:91], v[124:127], v[8:11], v[88:91]\n"
        "v_rcp_f32 v120, v120\n"
        "v_mfma_f32_16x16x32_bf16 v[88:91], v[124:127], v[16:19], v[88:91]\n"
        "s_waitcnt lgkmcnt(1)\n"
        "buffer_load_dwordx4 v114, s[44:47], s68 offen  lds\n"
        "v_mfma_f32_16x16x32_bf16 v[0:3], a[0:3], v[84:87], 0\n"
        "v_cvt_pk_bf16_f32 v24, v24, v25\n"
        "v_fma_f32 v27, v27, s52, v119\n"
        "v_mfma_f32_16x16x32_bf16 v[0:3], a[4:7], v[80:83], v[0:3]\n"
        "v_exp_f32 v26, v26\n"
        "v_mfma_f32_16x16x32_bf16 v[0:3], a[8:11], v[96:99], v[0:3]\n"
        "v_exp_f32 v27, v27\n"
        "v_mfma_f32_16x16x32_bf16 v[0:3], a[12:15], v[100:103], v[0:3]\n"
        "s_waitcnt lgkmcnt(0)\n"
        "ds_write_b32 v113, v88 offset:512\n"
        "v_mfma_f32_16x16x32_bf16 v[4:7], a[32:35], v[84:87], 0\n"
        "s_add_u32 m0, s67, 544\n"
        "v_add_f32 v121, v92, v93\n"
        "v_add_f32 v94, v94, v95\n"
        "v_add_f32 v121, v121, v94\n"
        "v_mfma_f32_16x16x32_bf16 v[4:7], a[36:39], v[80:83], v[4:7]\n"
        "buffer_load_dwordx4 v114, s[44:47], s68 offen offset:512  lds\n"
        "v_mfma_f32_16x16x32_bf16 v[4:7], a[40:43], v[96:99], v[4:7]\n"
        "s_add_u32 m0, s67, 1088\n"
        "v_cvt_pk_bf16_f32 v25, v26, v27\n"
        "v_fma_f32 v28, v28, s52, v119\n"
        "v_mfma_f32_16x16x32_bf16 v[4:7], a[44:47], v[100:103], v[4:7]\n"
        "buffer_load_dwordx4 v114, s[44:47], s68 offen offset:1024  lds\n"
        "v_mfma_f32_16x16x32_bf16 v[8:11], a[64:67], v[84:87], 0\n"
        "s_add_u32 m0, s67, 1632\n"
        "v_exp_f32 v28, v28\n"
        "v_mfma_f32_16x16x32_bf16 v[8:11], a[68:71], v[80:83], v[8:11]\n"
        "buffer_load_dwordx4 v114, s[44:47], s68 offen offset:1536  lds\n"
        "v_mfma_f32_16x16x32_bf16 v[8:11], a[72:75], v[96:99], v[8:11]\n"
        "v_rcp_f32 v121, v121\n"
        "v_mfma_f32_16x16x32_bf16 v[8:11], a[76:79], v[100:103], v[8:11]\n"
        "v_fma_f32 v29, v29, s52, v119\n"
        "v_fma_f32 v30, v30, s52, v119\n"
        "v_mfma_f32_16x16x32_bf16 v[12:15], a[96:99], v[84:87], 0\n"
        "v_exp_f32 v29, v29\n"
        "v_mfma_f32_16x16x32_bf16 v[12:15], a[100:103], v[80:83], v[12:15]\n"
        "v_cvt_pk_bf16_f32 v26, v28, v29\n"
        "v_fma_f32 v31, v31, s52, v119\n"
        "v_mfma_f32_16x16x32_bf16 v[12:15], a[104:107], v[96:99], v[12:15]\n"
        "ds_read_b128 v[92:95], v108 offset:128\n"
        "v_fma_f32 v32, v32, s52, v119\n"
        "v_mfma_f32_16x16x32_bf16 v[12:15], a[108:111], v[100:103], v[12:15]\n"
        "ds_read_b128 v[88:91], v108 offset:160\n"
        "ds_write_b128 v109, v[16:19] offset:2048\n"
        "v_mfma_f32_16x16x32_bf16 v[16:19], a[128:131], v[84:87], 0\n"
        "ds_read_b128 v[104:107], v108 offset:192\n"
        "v_fma_f32 v33, v33, s52, v119\n"
        "v_mfma_f32_16x16x32_bf16 v[16:19], a[132:135], v[80:83], v[16:19]\n"
        "s_waitcnt vmcnt(8)\n"
        "v_exp_f32 v30, v30\n"
        "v_mfma_f32_16x16x32_bf16 v[16:19], a[136:139], v[96:99], v[16:19]\n"
        "ds_read_b64_tr_b16 v[176:177], v115 offset:4224\n"
        "ds_read_b64_tr_b16 v[178:179], v115 offset:4736\n"
        "v_mfma_f32_16x16x32_bf16 v[16:19], a[140:143], v[100:103], v[16:19]\n"
        "s_mov_b32 s64, s61\n"
        "ds_read_b64_tr_b16 v[180:181], v115 offset:4256\n"
        "ds_read_b64_tr_b16 v[182:183], v115 offset:4768\n"
        "v_mfma_f32_16x16x32_bf16 v[20:23], a[160:163], v[84:87], 0\n"
        "s_add_u32 s61, s61, 16640\n"
        "ds_read_b128 v[84:87], v108 offset:224\n"
        "ds_read_b64_tr_b16 v[184:185], v115 offset:4288\n"
        "v_mfma_f32_16x16x32_bf16 v[20:23], a[164:167], v[80:83], v[20:23]\n"
        "s_cmp_eq_u32 s61, 108288\n"
        "s_cselect_b32 s61, 58368, s61\n"
        "ds_read_b64_tr_b16 v[186:187], v115 offset:4800\n"
        "ds_read_b64_tr_b16 v[188:189], v115 offset:4320\n"
        "v_mfma_f32_16x16x32_bf16 v[20:23], a[168:171], v[96:99], v[20:23]\n"
        "s_sub_u32 s62, s61, s64\n"
        "ds_read_b64_tr_b16 v[190:191], v115 offset:4832\n"
        "v_fma_f32 v34, v34, s52, v119\n"
        "v_mfma_f32_16x16x32_bf16 v[20:23], a[172:175], v[100:103], v[20:23]\n"
        "s_waitcnt lgkmcnt(9)\n"
        "v_exp_f32 v31, v31\n"
        "v_mfma_f32_16x16x32_bf16 v[0:3], a[16:19], v[92:95], v[0:3]\n"
        "v_cvt_pk_bf16_f32 v27, v30, v31\n"
        "v_fma_f32 v35, v35, s52, v119\n"
        "v_mfma_f32_16x16x32_bf16 v[0:3], a[20:23], v[88:91], v[0:3]\n"
        "s_waitcnt lgkmcnt(4)\n"
        "ds_write_b128 v109, v[24:27] offset:3072\n"
        "v_mfma_f32_16x16x32_bf16 v[0:3], a[24:27], v[104:107], v[0:3]\n"
        "v_exp_f32 v32, v32\n"
        "v_mfma_f32_16x16x32_bf16 v[0:3], a[28:31], v[84:87], v[0:3]\n"
        "s_waitcnt lgkmcnt(1)\n"
        "v_exp_f32 v33, v33\n"
        "v_mfma_f32_16x16x32_bf16 v[4:7], a[48:51], v[92:95], v[4:7]\n"
        "s_add_u32 s68, s75, 16384\n"
        "v_cvt_pk_bf16_f32 v32, v32, v33\n"
        "v_fma_f32 v36, v36, s52, v119\n"
        "v_mfma_f32_16x16x32_bf16 v[4:7], a[52:55], v[88:91], v[4:7]\n"
        "s_add_u32 m0, s67, 4224\n"
        "v_exp_f32 v34, v34\n"
        "v_mfma_f32_16x16x32_bf16 v[4:7], a[56:59], v[104:107], v[4:7]\n"
        "buffer_load_dwordx4 v114, s[44:47], s68 offen  lds\n"
        "v_mfma_f32_16x16x32_bf16 v[4:7], a[60:63], v[84:87], v[4:7]\n"
        "s_add_u32 m0, s67, 4768\n"
        "v_exp_f32 v35, v35\n"
        "v_mfma_f32_16x16x32_bf16 v[8:11], a[80:83], v[92:95], v[8:11]\n"
        "buffer_load_dwordx4 v114, s[44:47], s68 offen offset:512  lds\n"
        "v_mfma_f32_16x16x32_bf16 v[8:11], a[84:87], v[88:91], v[8:11]\n"
        "s_add_u32 m0, s67, 5312\n"
        "v_cvt_pk_bf16_f32 v33, v34, v35\n"
        "v_fma_f32 v37, v37, s52, v119\n"
        "v_mfma_f32_16x16x32_bf16 v[8:11], a[88:91], v[104:107], v[8:11]\n"
        "buffer_load_dwordx4 v114, s[44:47], s68 offen offset:1024  lds\n"
        "v_mfma_f32_16x16x32_bf16 v[8:11], a[92:95], v[84:87], v[8:11]\n"
        "s_add_u32 m0, s67, 5856\n"
        "v_exp_f32 v36, v36\n"
        "v_mfma_f32_16x16x32_bf16 v[12:15], a[112:115], v[92:95], v[12:15]\n"
        "buffer_load_dwordx4 v114, s[44:47], s68 offen offset:1536  lds\n"
        "v_mfma_f32_16x16x32_bf16 v[12:15], a[116:119], v[88:91], v[12:15]\n"
        "v_exp_f32 v37, v37\n"
        "v_mfma_f32_16x16x32_bf16 v[12:15], a[120:123], v[104:107], v[12:15]\n"
        "v_cvt_pk_bf16_f32 v34, v36, v37\n"
        "v_fma_f32 v38, v38, s52, v119\n"
        "v_mfma_f32_16x16x32_bf16 v[12:15], a[124:127], v[84:87], v[12:15]\n"
        "v_exp_f32 v38, v38\n"
        "v_mfma_f32_16x16x32_bf16 v[16:19], a[144:147], v[92:95], v[16:19]\n"
        "v_fma_f32 v39, v39, s52, v119\n"
        "v_fma_f32 v40, v40, s52, v119\n"
        "v_mfma_f32_16x16x32_bf16 v[16:19], a[148:151], v[88:91], v[16:19]\n"
        "ds_read_b128 v[100:103], v108 offset:512\n"
        "v_fma_f32 v41, v41, s52, v119\n"
        "v_mfma_f32_16x16x32_bf16 v[16:19], a[152:155], v[104:107], v[16:19]\n"
        "ds_read_b128 v[96:99], v108 offset:544\n"
        "v_fma_f32 v42, v42, s52, v119\n"
        "v_mfma_f32_16x16x32_bf16 v[16:19], a[156:159], v[84:87], v[16:19]\n"
        "s_waitcnt vmcnt(8)\n"
        "ds_read_b128 v[80:83], v108 offset:576\n"
        "ds_read_b64_tr_b16 v[192:193], v115 offset:8448\n"
        "v_mfma_f32_16x16x32_bf16 v[20:23], a[176:179], v[92:95], v[20:23]\n"
        "ds_read_b128 v[92:95], v108 offset:608\n"
        "ds_read_b64_tr_b16 v[194:195], v115 offset:8960\n"
        "v_mfma_f32_16x16x32_bf16 v[20:23], a[180:183], v[88:91], v[20:23]\n"
        "v_exp_f32 v39, v39\n"
        "v_exp_f32 v40, v40\n"
        "v_exp_f32 v41, v41\n"
        "v_exp_f32 v42, v42\n"
        "v_fma_f32 v43, v43, s52, v119\n"
        "v_exp_f32 v43, v43\n"
        "v_fma_f32 v44, v44, s52, v119\n"
        "v_exp_f32 v44, v44\n"
        "v_fma_f32 v45, v45, s52, v119\n"
        "v_exp_f32 v45, v45\n"
        "v_fma_f32 v46, v46, s52, v119\n"
        "v_exp_f32 v46, v46\n"
        "v_fma_f32 v47, v47, s52, v119\n"
        "v_exp_f32 v47, v47\n"
        "v_mfma_f32_16x16x32_bf16 v[20:23], a[184:187], v[104:107], v[20:23]\n"
        "v_cvt_pk_bf16_f32 v35, v38, v39\n"
        "v_cvt_pk_bf16_f32 v40, v40, v41\n"
        "v_cvt_pk_bf16_f32 v41, v42, v43\n"
        "v_cvt_pk_bf16_f32 v42, v44, v45\n"
        "v_cvt_pk_bf16_f32 v43, v46, v47\n"
        "v_mfma_f32_16x16x32_bf16 v[20:23], a[188:191], v[84:87], v[20:23]\n"
        "ds_read_b64_tr_b16 v[196:197], v115 offset:8480\n"
        "ds_read_b64_tr_b16 v[198:199], v115 offset:8992\n"
        "v_mfma_f32_16x16x32_bf16 v[84:87], v[124:127], v[24:27], 0\n"
        "ds_read_b64_tr_b16 v[200:201], v115 offset:8512\n"
        "ds_read_b64_tr_b16 v[202:203], v115 offset:9024\n"
        "v_mfma_f32_16x16x32_bf16 v[84:87], v[124:127], v[32:35], v[84:87]\n"
        "ds_read_b64_tr_b16 v[204:205], v115 offset:8544\n"
        "ds_read_b64_tr_b16 v[206:207], v115 offset:9056\n"
        "v_mfma_f32_16x16x32_bf16 v[84:87], v[124:127], v[40:43], v[84:87]\n"
        "s_waitcnt lgkmcnt(7)\n"
        "v_max_f32 v118, v0, v1\n"
        "v_max3_f32 v118, v118, v2, v3\n"
        "v_mfma_f32_16x16x32_bf16 v[24:27], a[0:3], v[100:103], 0\n"
        "ds_write_b128 v109, v[32:35] offset:4096\n"
        "ds_write_b128 v109, v[40:43] offset:5120\n"
        "v_mfma_f32_16x16x32_bf16 v[24:27], a[4:7], v[96:99], v[24:27]\n"
        "v_max3_f32 v118, v118, v4, v5\n"
        "v_max3_f32 v118, v118, v6, v7\n"
        "v_mfma_f32_16x16x32_bf16 v[24:27], a[8:11], v[80:83], v[24:27]\n"
        "v_max3_f32 v118, v118, v8, v9\n"
        "v_max3_f32 v118, v118, v10, v11\n"
        "v_mfma_f32_16x16x32_bf16 v[24:27], a[12:15], v[92:95], v[24:27]\n"
        "s_waitcnt lgkmcnt(2)\n"
        "ds_write_b32 v113, v84 offset:768\n"
        "v_mfma_f32_16x16x32_bf16 v[28:31], a[32:35], v[100:103], 0\n"
        "s_add_u32 s68, s75, 32768\n"
        "v_max3_f32 v118, v118, v12, v13\n"
        "v_max3_f32 v118, v118, v14, v15\n"
        "v_mfma_f32_16x16x32_bf16 v[28:31], a[36:39], v[96:99], v[28:31]\n"
        "s_add_u32 m0, s67, 8448\n"
        "v_max3_f32 v118, v118, v16, v17\n"
        "v_max3_f32 v118, v118, v18, v19\n"
        "v_mfma_f32_16x16x32_bf16 v[28:31], a[40:43], v[80:83], v[28:31]\n"
        "buffer_load_dwordx4 v114, s[44:47], s68 offen  lds\n"
        "v_mfma_f32_16x16x32_bf16 v[28:31], a[44:47], v[92:95], v[28:31]\n"
        "s_add_u32 m0, s67, 8992\n"
        "v_max3_f32 v118, v118, v20, v21\n"
        "v_max3_f32 v118, v118, v22, v23\n"
        "v_mfma_f32_16x16x32_bf16 v[32:35], a[64:67], v[100:103], 0\n"
        "buffer_load_dwordx4 v114, s[44:47], s68 offen offset:512  lds\n"
        "v_mfma_f32_16x16x32_bf16 v[32:35], a[68:71], v[96:99], v[32:35]\n"
        "s_add_u32 m0, s67, 9536\n"
        "v_xor_b32 v112, 0x8000, v112\n"
        "v_xor_b32 v113, 0x8000, v113\n"
        "v_mfma_f32_16x16x32_bf16 v[32:35], a[72:75], v[80:83], v[32:35]\n"
        "buffer_load_dwordx4 v114, s[44:47], s68 offen offset:1024  lds\n"
        "v_mfma_f32_16x16x32_bf16 v[32:35], a[76:79], v[92:95], v[32:35]\n"
        "s_add_u32 m0, s67, 10080\n"
        "v_mfma_f32_16x16x32_bf16 v[36:39], a[96:99], v[100:103], 0\n"
        "buffer_load_dwordx4 v114, s[44:47], s68 offen offset:1536  lds\n"
        "v_mfma_f32_16x16x32_bf16 v[36:39], a[100:103], v[96:99], v[36:39]\n"
        "s_add_u32 m0, s69, 58368\n"
        "v_mfma_f32_16x16x32_bf16 v[36:39], a[104:107], v[80:83], v[36:39]\n"
        "ds_read_b128 v[84:87], v108 offset:640\n"
        "v_mfma_f32_16x16x32_bf16 v[36:39], a[108:111], v[92:95], v[36:39]\n"
        "ds_read_b128 v[104:107], v108 offset:672\n"
        "v_mfma_f32_16x16x32_bf16 v[40:43], a[128:131], v[100:103], 0\n"
        "ds_read_b128 v[88:91], v108 offset:704\n"
        "v_mfma_f32_16x16x32_bf16 v[40:43], a[132:135], v[96:99], v[40:43]\n"
        "buffer_load_dwordx4 v117, s[36:39], s54 offen nt lds\n"
        "v_mfma_f32_16x16x32_bf16 v[40:43], a[136:139], v[80:83], v[40:43]\n"
        "s_add_u32 m0, m0, 528\n"
        "v_mfma_f32_16x16x32_bf16 v[40:43], a[140:143], v[92:95], v[40:43]\n"
        "buffer_load_dwordx4 v117, s[36:39], s54 offen offset:512 nt lds\n"
        "v_mfma_f32_16x16x32_bf16 v[44:47], a[160:163], v[100:103], 0\n"
        "s_add_u32 m0, m0, 528\n"
        "ds_read_b128 v[100:103], v108 offset:736\n"
        "v_add_u32 v108, s62, v108\n"
        "v_mfma_f32_16x16x32_bf16 v[44:47], a[164:167], v[96:99], v[44:47]\n"
        "buffer_load_dwordx4 v117, s[36:39], s54 offen offset:1024 nt lds\n"
        "v_mfma_f32_16x16x32_bf16 v[44:47], a[168:171], v[80:83], v[44:47]\n"
        "s_add_u32 m0, m0, 528\n"
        "v_mfma_f32_16x16x32_bf16 v[44:47], a[172:175], v[92:95], v[44:47]\n"
        "s_waitcnt lgkmcnt(1)\n"
        "buffer_load_dwordx4 v117, s[36:39], s54 offen offset:1536 nt lds\n"
        "v_mfma_f32_16x16x32_bf16 v[24:27], a[16:19], v[84:87], v[24:27]\n"
        "v_mfma_f32_16x16x32_bf16 v[24:27], a[20:23], v[104:107], v[24:27]\n"
        "s_waitcnt lgkmcnt(0)\n"
        "v_mfma_f32_16x16x32_bf16 v[24:27], a[24:27], v[88:91], v[24:27]\n"
        "v_mfma_f32_16x16x32_bf16 v[24:27], a[28:31], v[100:103], v[24:27]\n"
        "s_add_u32 s54, s54, 16384\n"
        "v_mfma_f32_16x16x32_bf16 v[28:31], a[48:51], v[84:87], v[28:31]\n"
        "v_mfma_f32_16x16x32_bf16 v[28:31], a[52:55], v[104:107], v[28:31]\n"
        "s_add_u32 m0, s55, s69\n"
        "v_mfma_f32_16x16x32_bf16 v[28:31], a[56:59], v[88:91], v[28:31]\n"
        "buffer_load_dwordx4 v117, s[36:39], s54 offen nt lds\n"
        "v_mfma_f32_16x16x32_bf16 v[28:31], a[60:63], v[100:103], v[28:31]\n"
        "s_add_u32 m0, m0, 528\n"
        "v_max_f32 v119, v24, v25\n"
        "v_max3_f32 v119, v119, v26, v27\n"
        "v_mfma_f32_16x16x32_bf16 v[32:35], a[80:83], v[84:87], v[32:35]\n"
        "s_waitcnt vmcnt(13)\n"
        "ds_read_b64_tr_b16 v[208:209], v115 offset:0\n"
        "ds_read_b64_tr_b16 v[210:211], v115 offset:512\n"
        "v_mfma_f32_16x16x32_bf16 v[32:35], a[84:87], v[104:107], v[32:35]\n"
        "ds_read_b64_tr_b16 v[212:213], v115 offset:32\n"
        "ds_read_b64_tr_b16 v[214:215], v115 offset:544\n"
        "v_mfma_f32_16x16x32_bf16 v[32:35], a[88:91], v[88:91], v[32:35]\n"
        "ds_read_b64_tr_b16 v[216:217], v115 offset:64\n"
        "ds_read_b64_tr_b16 v[218:219], v115 offset:576\n"
        "v_mfma_f32_16x16x32_bf16 v[32:35], a[92:95], v[100:103], v[32:35]\n"
        "ds_read_b64_tr_b16 v[220:221], v115 offset:96\n"
        "ds_read_b64_tr_b16 v[222:223], v115 offset:608\n"
        "v_mfma_f32_16x16x32_bf16 v[36:39], a[112:115], v[84:87], v[36:39]\n"
        "buffer_load_dwordx4 v117, s[36:39], s54 offen offset:512 nt lds\n"
        "v_mfma_f32_16x16x32_bf16 v[36:39], a[116:119], v[104:107], v[36:39]\n"
        "s_add_u32 m0, m0, 528\n"
        "v_max3_f32 v119, v119, v28, v29\n"
        "v_max3_f32 v119, v119, v30, v31\n"
        "v_mfma_f32_16x16x32_bf16 v[36:39], a[120:123], v[88:91], v[36:39]\n"
        "ds_read_b128 v[92:95], v110 offset:0\n"
        "v_max3_f32 v119, v119, v32, v33\n"
        "v_mfma_f32_16x16x32_bf16 v[36:39], a[124:127], v[100:103], v[36:39]\n"
        "s_waitcnt lgkmcnt(1)\n"
        "ds_read_b128 v[80:83], v110 offset:1024\n"
        "v_max3_f32 v119, v119, v34, v35\n"
        "v_mfma_f32_16x16x32_bf16 v[40:43], a[144:147], v[84:87], v[40:43]\n"
        "ds_read_b128 v[96:99], v110 offset:2048\n"
        "v_mfma_f32_16x16x32_bf16 v[40:43], a[148:151], v[104:107], v[40:43]\n"
        "buffer_load_dwordx4 v117, s[36:39], s54 offen offset:1024 nt lds\n"
        "v_mfma_f32_16x16x32_bf16 v[40:43], a[152:155], v[88:91], v[40:43]\n"
        "s_add_u32 m0, m0, 528\n"
        "v_max3_f32 v119, v119, v36, v37\n"
        "v_max3_f32 v119, v119, v38, v39\n"
        "v_mfma_f32_16x16x32_bf16 v[40:43], a[156:159], v[100:103], v[40:43]\n"
        "buffer_load_dwordx4 v117, s[36:39], s54 offen offset:1536 nt lds\n"
        "v_mfma_f32_16x16x32_bf16 v[44:47], a[176:179], v[84:87], v[44:47]\n"
        "s_add_u32 s54, s54, 16384\n"
        "v_mfma_f32_16x16x32_bf16 v[44:47], a[180:183], v[104:107], v[44:47]\n"
        "s_mov_b32 s55, s64\n"
        "v_mfma_f32_16x16x32_bf16 v[44:47], a[184:187], v[88:91], v[44:47]\n"
        "v_max3_f32 v119, v119, v40, v41\n"
        "v_max3_f32 v119, v119, v42, v43\n"
        "v_mfma_f32_16x16x32_bf16 v[44:47], a[188:191], v[100:103], v[44:47]\n"
        "s_waitcnt lgkmcnt(0)\n"
        "v_mfma_f32_16x16x32_bf16 v[48:51], a[240:243], v[92:95], v[48:51]\n"
        "v_mfma_f32_16x16x32_bf16 v[48:51], v[128:131], v[80:83], v[48:51]\n"
        "v_mfma_f32_16x16x32_bf16 v[48:51], v[144:147], v[96:99], v[48:51]\n"
        "ds_read_b128 v[100:103], v110 offset:3072\n"
        "v_max3_f32 v119, v119, v44, v45\n"
        "v_mfma_f32_16x16x32_bf16 v[52:55], a[244:247], v[92:95], v[52:55]\n"
        "ds_read_b128 v[88:91], v110 offset:4096\n"
        "v_max3_f32 v119, v119, v46, v47\n"
        "v_mfma_f32_16x16x32_bf16 v[52:55], v[132:135], v[80:83], v[52:55]\n"
        "ds_read_b128 v[104:107], v110 offset:5120\n"
        "v_add_u32 v110, s58, v110\n"
        "v_mfma_f32_16x16x32_bf16 v[52:55], v[148:151], v[96:99], v[52:55]\n"
        "v_permlane32_swap_b32 v118, v119\n"
        "v_mfma_f32_16x16x32_bf16 v[56:59], a[248:251], v[92:95], v[56:59]\n"
        "v_max_f32 v118, v118, v119\n"
        "v_mov_b32 v119, v118\n"
        "v_mfma_f32_16x16x32_bf16 v[56:59], v[136:139], v[80:83], v[56:59]\n"
        "v_permlane16_swap_b32 v118, v119\n"
        "v_mfma_f32_16x16x32_bf16 v[56:59], v[152:155], v[96:99], v[56:59]\n"
        "v_max_f32 v118, v118, v119\n"
        "v_mfma_f32_16x16x32_bf16 v[60:63], a[252:255], v[92:95], v[60:63]\n"
        "ds_write_b32 v111, v118 offset:0\n"
        "v_mfma_f32_16x16x32_bf16 v[60:63], v[140:143], v[80:83], v[60:63]\n"
        "v_xor_b32 v111, 0x8000, v111\n"
        "v_mfma_f32_16x16x32_bf16 v[60:63], v[156:159], v[96:99], v[60:63]\n"
        "s_waitcnt lgkmcnt(1)\n"
        "v_mfma_f32_16x16x32_bf16 v[64:67], a[240:243], v[100:103], v[64:67]\n"
        "s_waitcnt vmcnt(12)\n"
        "ds_read_b64_tr_b16 v[224:225], v115 offset:4224\n"
        "v_mfma_f32_16x16x32_bf16 v[64:67], v[128:131], v[88:91], v[64:67]\n"
        "ds_read_b64_tr_b16 v[226:227], v115 offset:4736\n"
        "ds_read_b64_tr_b16 v[228:229], v115 offset:4256\n"
        "v_mfma_f32_16x16x32_bf16 v[64:67], v[144:147], v[104:107], v[64:67]\n"
        "ds_read_b128 v[96:99], v110 offset:0\n"
        "ds_read_b64_tr_b16 v[230:231], v115 offset:4768\n"
        "v_mfma_f32_16x16x32_bf16 v[68:71], a[244:247], v[100:103], v[68:71]\n"
        "ds_read_b128 v[80:83], v110 offset:1024\n"
        "ds_read_b64_tr_b16 v[232:233], v115 offset:4288\n"
        "v_mfma_f32_16x16x32_bf16 v[68:71], v[132:135], v[88:91], v[68:71]\n"
        "ds_read_b128 v[92:95], v110 offset:2048\n"
        "ds_read_b64_tr_b16 v[234:235], v115 offset:4800\n"
        "v_mfma_f32_16x16x32_bf16 v[68:71], v[148:151], v[104:107], v[68:71]\n"
        "ds_read_b64_tr_b16 v[236:237], v115 offset:4320\n"
        "ds_read_b64_tr_b16 v[238:239], v115 offset:4832\n"
        "v_mfma_f32_16x16x32_bf16 v[72:75], a[248:251], v[100:103], v[72:75]\n"
        "v_mfma_f32_16x16x32_bf16 v[72:75], v[136:139], v[88:91], v[72:75]\n"
        "v_mfma_f32_16x16x32_bf16 v[72:75], v[152:155], v[104:107], v[72:75]\n"
        "v_mfma_f32_16x16x32_bf16 v[76:79], a[252:255], v[100:103], v[76:79]\n"
        "s_waitcnt lgkmcnt(0)\n"
        "v_mfma_f32_16x16x32_bf16 v[76:79], v[140:143], v[88:91], v[76:79]\n"
        "v_mfma_f32_16x16x32_bf16 v[76:79], v[156:159], v[104:107], v[76:79]\n"
        "v_mfma_f32_16x16x32_bf16 v[48:51], v[160:163], v[96:99], v[48:51]\n"
        "v_mfma_f32_16x16x32_bf16 v[48:51], v[176:179], v[80:83], v[48:51]\n"
        "v_mfma_f32_16x16x32_bf16 v[48:51], v[192:195], v[92:95], v[48:51]\n"
        "ds_read_b128 v[104:107], v110 offset:3072\n"
        "v_mfma_f32_16x16x32_bf16 v[52:55], v[164:167], v[96:99], v[52:55]\n"
        "ds_read_b128 v[88:91], v110 offset:4096\n"
        "v_mfma_f32_16x16x32_bf16 v[52:55], v[180:183], v[80:83], v[52:55]\n"
        "ds_read_b128 v[100:103], v110 offset:5120\n"
        "v_add_u32 v110, s59, v110\n"
        "v_mfma_f32_16x16x32_bf16 v[52:55], v[196:199], v[92:95], v[52:55]\n"
        "v_mfma_f32_16x16x32_bf16 v[56:59], v[168:171], v[96:99], v[56:59]\n"
        "v_mfma_f32_16x16x32_bf16 v[56:59], v[184:187], v[80:83], v[56:59]\n"
        "v_mfma_f32_16x16x32_bf16 v[56:59], v[200:203], v[92:95], v[56:59]\n"
        "v_mfma_f32_16x16x32_bf16 v[60:63], v[172:175], v[96:99], v[60:63]\n"
        "v_mfma_f32_16x16x32_bf16 v[60:63], v[188:191], v[80:83], v[60:63]\n"
        "v_mfma_f32_16x16x32_bf16 v[60:63], v[204:207], v[92:95], v[60:63]\n"
        "s_waitcnt lgkmcnt(0)\n"
        "v_mfma_f32_16x16x32_bf16 v[64:67], v[160:163], v[104:107], v[64:67]\n"
        "v_mfma_f32_16x16x32_bf16 v[64:67], v[176:179], v[88:91], v[64:67]\n"
        "v_mfma_f32_16x16x32_bf16 v[64:67], v[192:195], v[100:103], v[64:67]\n"
        "s_waitcnt vmcnt(8)\n"
        "ds_read_b128 v[92:95], v110 offset:0\n"
        "ds_read_b64_tr_b16 v[240:241], v115 offset:8448\n"
        "v_mfma_f32_16x16x32_bf16 v[68:71], v[164:167], v[104:107], v[68:71]\n"
        "ds_read_b128 v[80:83], v110 offset:1024\n"
        "ds_read_b64_tr_b16 v[242:243], v115 offset:8960\n"
        "v_mfma_f32_16x16x32_bf16 v[68:71], v[180:183], v[88:91], v[68:71]\n"
        "ds_read_b128 v[96:99], v110 offset:2048\n"
        "ds_read_b64_tr_b16 v[244:245], v115 offset:8480\n"
        "v_mfma_f32_16x16x32_bf16 v[68:71], v[196:199], v[100:103], v[68:71]\n"
        "ds_read_b64_tr_b16 v[246:247], v115 offset:8992\n"
        "ds_read_b64_tr_b16 v[248:249], v115 offset:8512\n"
        "v_mfma_f32_16x16x32_bf16 v[72:75], v[168:171], v[104:107], v[72:75]\n"
        "ds_read_b64_tr_b16 v[250:251], v115 offset:9024\n"
        "ds_read_b64_tr_b16 v[252:253], v115 offset:8544\n"
        "v_mfma_f32_16x16x32_bf16 v[72:75], v[184:187], v[88:91], v[72:75]\n"
        "ds_read_b64_tr_b16 v[254:255], v115 offset:9056\n"
        "v_mbcnt_lo_u32_b32 v122, -1, 0\n"
        "v_mfma_f32_16x16x32_bf16 v[72:75], v[200:203], v[100:103], v[72:75]\n"
        "v_mbcnt_hi_u32_b32 v122, -1, v122\n"
        "v_and_b32 v123, 15, v122\n"
        "v_mfma_f32_16x16x32_bf16 v[76:79], v[172:175], v[104:107], v[76:79]\n"
        "v_mul_u32_u24 v114, 144, v123\n"
        "v_lshrrev_b32 v123, 4, v122\n"
        "v_mfma_f32_16x16x32_bf16 v[76:79], v[188:191], v[88:91], v[76:79]\n"
        "v_lshl_add_u32 v114, v123, 3, v114\n"
        "v_add_u32 v114, s67, v114\n"
        "v_mfma_f32_16x16x32_bf16 v[76:79], v[204:207], v[100:103], v[76:79]\n"
        "s_waitcnt lgkmcnt(6)\n"
        "s_waitcnt lgkmcnt(0)\n"
        "v_lshrrev_b32 v123, 3, v122\n"
        "v_mfma_f32_16x16x32_bf16 v[48:51], v[208:211], v[92:95], v[48:51]\n"
        "v_and_b32 v122, 7, v122\n"
        "v_lshlrev_b32 v122, 4, v122\n"
        "v_mfma_f32_16x16x32_bf16 v[48:51], v[224:227], v[80:83], v[48:51]\n"
        "v_lshl_add_u32 v116, v123, 9, v122\n"
        "v_mul_u32_u24 v123, 144, v123\n"
        "v_mfma_f32_16x16x32_bf16 v[48:51], v[240:243], v[96:99], v[48:51]\n"
        "ds_read_b128 v[100:103], v110 offset:3072\n"
        "v_add3_u32 v115, v123, v122, s67\n"
        "v_mfma_f32_16x16x32_bf16 v[52:55], v[212:215], v[92:95], v[52:55]\n"
        "ds_read_b128 v[88:91], v110 offset:4096\n"
        "v_mfma_f32_16x16x32_bf16 v[52:55], v[228:231], v[80:83], v[52:55]\n"
        "ds_read_b128 v[104:107], v110 offset:5120\n"
        "v_mfma_f32_16x16x32_bf16 v[52:55], v[244:247], v[96:99], v[52:55]\n"
        "v_mul_f32 v48, v120, v48\n"
        "v_mul_f32 v49, v120, v49\n"
        "v_mfma_f32_16x16x32_bf16 v[56:59], v[216:219], v[92:95], v[56:59]\n"
        "v_mul_f32 v50, v120, v50\n"
        "v_mul_f32 v51, v120, v51\n"
        "v_mfma_f32_16x16x32_bf16 v[56:59], v[232:235], v[80:83], v[56:59]\n"
        "v_cvt_pk_bf16_f32 v48, v48, v49\n"
        "v_cvt_pk_bf16_f32 v49, v50, v51\n"
        "v_mfma_f32_16x16x32_bf16 v[56:59], v[248:251], v[96:99], v[56:59]\n"
        "ds_write_b64 v114, v[48:49] offset:0\n"
        "v_mfma_f32_16x16x32_bf16 v[60:63], v[220:223], v[92:95], v[60:63]\n"
        "v_mul_f32 v52, v120, v52\n"
        "v_mul_f32 v53, v120, v53\n"
        "v_mfma_f32_16x16x32_bf16 v[60:63], v[236:239], v[80:83], v[60:63]\n"
        "v_mul_f32 v54, v120, v54\n"
        "v_mul_f32 v55, v120, v55\n"
        "v_mfma_f32_16x16x32_bf16 v[60:63], v[252:255], v[96:99], v[60:63]\n"
        "s_waitcnt lgkmcnt(1)\n"
        "v_cvt_pk_bf16_f32 v52, v52, v53\n"
        "v_cvt_pk_bf16_f32 v53, v54, v55\n"
        "v_mfma_f32_16x16x32_bf16 v[64:67], v[208:211], v[100:103], v[64:67]\n"
        "ds_write_b64 v114, v[52:53] offset:32\n"
        "v_mfma_f32_16x16x32_bf16 v[64:67], v[224:227], v[88:91], v[64:67]\n"
        "v_mul_f32 v56, v120, v56\n"
        "v_mul_f32 v57, v120, v57\n"
        "v_mfma_f32_16x16x32_bf16 v[64:67], v[240:243], v[104:107], v[64:67]\n"
        "v_mul_f32 v58, v120, v58\n"
        "v_mul_f32 v59, v120, v59\n"
        "v_mfma_f32_16x16x32_bf16 v[68:71], v[212:215], v[100:103], v[68:71]\n"
        "v_cvt_pk_bf16_f32 v56, v56, v57\n"
        "v_cvt_pk_bf16_f32 v57, v58, v59\n"
        "v_mfma_f32_16x16x32_bf16 v[68:71], v[228:231], v[88:91], v[68:71]\n"
        "v_mul_f32 v60, v120, v60\n"
        "v_mul_f32 v61, v120, v61\n"
        "v_mfma_f32_16x16x32_bf16 v[68:71], v[244:247], v[104:107], v[68:71]\n"
        "ds_read_b128 v[96:99], v109 offset:0\n"
        "v_mul_f32 v62, v120, v62\n"
        "v_mfma_f32_16x16x32_bf16 v[72:75], v[216:219], v[100:103], v[72:75]\n"
        "ds_read_b128 v[80:83], v109 offset:1024\n"
        "v_mul_f32 v63, v120, v63\n"
        "v_cvt_pk_bf16_f32 v60, v60, v61\n"
        "v_cvt_pk_bf16_f32 v61, v62, v63\n"
        "v_mfma_f32_16x16x32_bf16 v[72:75], v[232:235], v[88:91], v[72:75]\n"
        "ds_read_b128 v[92:95], v109 offset:2048\n"
        "ds_write_b64 v114, v[56:57] offset:64\n"
        "ds_write_b64 v114, v[60:61] offset:96\n"
        "ds_read_b128 v[48:51], v115 offset:0\n"
        "v_mfma_f32_16x16x32_bf16 v[72:75], v[248:251], v[104:107], v[72:75]\n"
        "ds_read_b128 v[52:55], v115 offset:1152\n"
        "v_mul_f32 v64, v121, v64\n"
        "v_mfma_f32_16x16x32_bf16 v[76:79], v[220:223], v[100:103], v[76:79]\n"
        "v_mul_f32 v65, v121, v65\n"
        "v_mul_f32 v66, v121, v66\n"
        "v_mfma_f32_16x16x32_bf16 v[76:79], v[236:239], v[88:91], v[76:79]\n"
        "v_mul_f32 v67, v121, v67\n"
        "v_cvt_pk_bf16_f32 v64, v64, v65\n"
        "v_mfma_f32_16x16x32_bf16 v[76:79], v[252:255], v[104:107], v[76:79]\n"
        "v_cvt_pk_bf16_f32 v65, v66, v67\n"
        "v_mul_f32 v68, v121, v68\n"
        "s_waitcnt lgkmcnt(1)\n"
        "buffer_store_dwordx4 v[48:51], v116, s[48:51], s56 offen sc1\n"
        "s_waitcnt lgkmcnt(0)\n"
        "s_add_u32 s56, s56, 4096\n"
        "buffer_store_dwordx4 v[52:55], v116, s[48:51], s56 offen sc1\n"
        "s_add_u32 s56, s56, 4096\n"
        "v_mul_f32 v69, v121, v69\n"
        "v_mul_f32 v70, v121, v70\n"
        "v_mul_f32 v71, v121, v71\n"
        "v_mfma_f32_16x16x32_bf16 v[48:51], a[192:195], v[96:99], 0\n"
        "v_cvt_pk_bf16_f32 v68, v68, v69\n"
        "v_cvt_pk_bf16_f32 v69, v70, v71\n"
        "v_mfma_f32_16x16x32_bf16 v[48:51], a[208:211], v[80:83], v[48:51]\n"
        "ds_read_b128 v[84:87], v109 offset:4096\n"
        "v_mul_f32 v72, v121, v72\n"
        "v_mfma_f32_16x16x32_bf16 v[48:51], a[224:227], v[92:95], v[48:51]\n"
        "ds_read_b128 v[88:91], v109 offset:5120\n"
        "v_mul_f32 v73, v121, v73\n"
        "v_mfma_f32_16x16x32_bf16 v[52:55], a[196:199], v[96:99], 0\n"
        "ds_write_b64 v114, v[64:65] offset:2304\n"
        "v_mfma_f32_16x16x32_bf16 v[52:55], a[212:215], v[80:83], v[52:55]\n"
        "v_mul_f32 v74, v121, v74\n"
        "v_mul_f32 v75, v121, v75\n"
        "v_mfma_f32_16x16x32_bf16 v[52:55], a[228:231], v[92:95], v[52:55]\n"
        "ds_write_b64 v114, v[68:69] offset:2336\n"
        "v_mfma_f32_16x16x32_bf16 v[56:59], a[200:203], v[96:99], 0\n"
        "v_cvt_pk_bf16_f32 v72, v72, v73\n"
        "v_cvt_pk_bf16_f32 v73, v74, v75\n"
        "v_mfma_f32_16x16x32_bf16 v[56:59], a[216:219], v[80:83], v[56:59]\n"
        "ds_write_b64 v114, v[72:73] offset:2368\n"
        "v_mfma_f32_16x16x32_bf16 v[56:59], a[232:235], v[92:95], v[56:59]\n"
        "v_mul_f32 v76, v121, v76\n"
        "v_mul_f32 v77, v121, v77\n"
        "v_mfma_f32_16x16x32_bf16 v[60:63], a[204:207], v[96:99], 0\n"
        "v_mul_f32 v78, v121, v78\n"
        "v_mul_f32 v79, v121, v79\n"
        "v_mfma_f32_16x16x32_bf16 v[60:63], a[220:223], v[80:83], v[60:63]\n"
        "ds_read_b128 v[80:83], v109 offset:3072\n"
        "v_cvt_pk_bf16_f32 v76, v76, v77\n"
        "v_mfma_f32_16x16x32_bf16 v[60:63], a[236:239], v[92:95], v[60:63]\n"
        "v_cvt_pk_bf16_f32 v77, v78, v79\n"
        "v_add_u32 v110, s57, v109\n"
        "ds_write_b64 v114, v[76:77] offset:2400\n"
        "ds_read_b128 v[64:67], v115 offset:2304\n"
        "ds_read_b128 v[68:71], v115 offset:3456\n"
        "v_xor_b32 v109, 0x8000, v109\n"
        "s_waitcnt lgkmcnt(1)\n"
        "buffer_store_dwordx4 v[64:67], v116, s[48:51], s56 offen sc1\n"
        "s_waitcnt lgkmcnt(0)\n"
        "s_add_u32 s56, s56, 4096\n"
        "buffer_store_dwordx4 v[68:71], v116, s[48:51], s56 offen sc1\n"
        "s_add_u32 s56, s56, 4096\n"
        "s_waitcnt vmcnt(8) lgkmcnt(0)\n"
        "s_barrier\n"
        "s_branch 2f\n"
        ".p2align 6\n"
        "1:\n"
        "v_mfma_f32_16x16x32_bf16 v[48:51], a[208:211], v[96:99], v[48:51]\n"
        "v_xor_b32 v109, 0x8000, v109\n"
        "v_mfma_f32_16x16x32_bf16 v[48:51], a[224:227], v[100:103], v[48:51]\n"
        "v_max3_f32 v119, v119, v44, v45\n"
        "v_max3_f32 v119, v119, v46, v47\n"
        "v_mfma_f32_16x16x32_bf16 v[52:55], a[196:199], v[104:107], 0\n"
        "v_permlane32_swap_b32 v118, v119\n"
        "v_mfma_f32_16x16x32_bf16 v[52:55], a[212:215], v[96:99], v[52:55]\n"
        "v_max_f32 v118, v118, v119\n"
        "v_mov_b32 v119, v118\n"
        "v_mfma_f32_16x16x32_bf16 v[52:55], a[228:231], v[100:103], v[52:55]\n"
        "v_permlane16_swap_b32 v118, v119\n"
        "v_mfma_f32_16x16x32_bf16 v[56:59], a[200:203], v[104:107], 0\n"
        "v_max_f32 v118, v118, v119\n"
        "v_mfma_f32_16x16x32_bf16 v[56:59], a[216:219], v[96:99], v[56:59]\n"
        "ds_write_b32 v111, v118 offset:0\n"
        "v_mfma_f32_16x16x32_bf16 v[56:59], a[232:235], v[100:103], v[56:59]\n"
        "v_xor_b32 v111, 0x8000, v111\n"
        "v_mfma_f32_16x16x32_bf16 v[60:63], a[204:207], v[104:107], 0\n"
        "v_mfma_f32_16x16x32_bf16 v[60:63], a[220:223], v[96:99], v[60:63]\n"
        "s_sub_u32 s53, s53, 1\n"
        "v_mfma_f32_16x16x32_bf16 v[60:63], a[236:239], v[100:103], v[60:63]\n"
        "s_waitcnt vmcnt(12) lgkmcnt(0)\n"
        "s_barrier\n"
        "s_cbranch_scc1 3f\n"
        "2:\n"
        "v_mfma_f32_16x16x32_bf16 v[64:67], a[192:195], v[80:83], 0\n"
        "ds_read_b128 v[92:95], v112 offset:0\n"
        "v_mfma_f32_16x16x32_bf16 v[64:67], a[208:211], v[84:87], v[64:67]\n"
        "v_mfma_f32_16x16x32_bf16 v[64:67], a[224:227], v[88:91], v[64:67]\n"
        "ds_read_b128 v[96:99], v110 offset:0\n"
        "v_mfma_f32_16x16x32_bf16 v[68:71], a[196:199], v[80:83], 0\n"
        "ds_read_b128 v[100:103], v110 offset:1024\n"
        "v_mfma_f32_16x16x32_bf16 v[68:71], a[212:215], v[84:87], v[68:71]\n"
        "ds_read_b128 v[104:107], v110 offset:2048\n"
        "v_add_u32 v110, s58, v110\n"
        "v_mfma_f32_16x16x32_bf16 v[68:71], a[228:231], v[88:91], v[68:71]\n"
        "s_waitcnt lgkmcnt(3)\n"
        "v_mfma_f32_16x16x32_bf16 v[72:75], a[200:203], v[80:83], 0\n"
        "v_max3_f32 v118, v92, v93, v94\n"
        "v_max_f32 v118, v118, v95\n"
        "v_mfma_f32_16x16x32_bf16 v[72:75], a[216:219], v[84:87], v[72:75]\n"
        "v_mul_f32 v118, -s52, v118\n"
        "v_fma_f32 v0, v0, s52, v118\n"
        "v_mfma_f32_16x16x32_bf16 v[72:75], a[232:235], v[88:91], v[72:75]\n"
        "v_exp_f32 v0, v0\n"
        "v_mfma_f32_16x16x32_bf16 v[76:79], a[204:207], v[80:83], 0\n"
        "v_fma_f32 v1, v1, s52, v118\n"
        "v_fma_f32 v2, v2, s52, v118\n"
        "v_mfma_f32_16x16x32_bf16 v[76:79], a[220:223], v[84:87], v[76:79]\n"
        "v_exp_f32 v1, v1\n"
        "v_mfma_f32_16x16x32_bf16 v[76:79], a[236:239], v[88:91], v[76:79]\n"
        "s_waitcnt lgkmcnt(0)\n"
        "ds_read_b128 v[88:91], v112 offset:256\n"
        "v_cvt_pk_bf16_f32 v0, v0, v1\n"
        "v_mfma_f32_16x16x32_bf16 v[48:51], a[240:243], v[96:99], v[48:51]\n"
        "v_exp_f32 v2, v2\n"
        "v_mfma_f32_16x16x32_bf16 v[48:51], v[128:131], v[100:103], v[48:51]\n"
        "v_fma_f32 v3, v3, s52, v118\n"
        "v_fma_f32 v4, v4, s52, v118\n"
        "v_mfma_f32_16x16x32_bf16 v[48:51], v[144:147], v[104:107], v[48:51]\n"
        "ds_read_b128 v[84:87], v110 offset:0\n"
        "v_fma_f32 v5, v5, s52, v118\n"
        "v_mfma_f32_16x16x32_bf16 v[52:55], a[244:247], v[96:99], v[52:55]\n"
        "ds_read_b128 v[80:83], v110 offset:1024\n"
        "v_fma_f32 v6, v6, s52, v118\n"
        "v_mfma_f32_16x16x32_bf16 v[52:55], v[132:135], v[100:103], v[52:55]\n"
        "ds_read_b128 v[92:95], v110 offset:2048\n"
        "v_add_u32 v110, s59, v110\n"
        "v_mfma_f32_16x16x32_bf16 v[52:55], v[148:151], v[104:107], v[52:55]\n"
        "s_waitcnt lgkmcnt(3)\n"
        "v_exp_f32 v3, v3\n"
        "v_mfma_f32_16x16x32_bf16 v[56:59], a[248:251], v[96:99], v[56:59]\n"
        "v_max3_f32 v119, v88, v89, v90\n"
        "v_max_f32 v119, v119, v91\n"
        "v_mfma_f32_16x16x32_bf16 v[56:59], v[136:139], v[100:103], v[56:59]\n"
        "v_mul_f32 v119, -s52, v119\n"
        "v_cvt_pk_bf16_f32 v1, v2, v3\n"
        "v_mfma_f32_16x16x32_bf16 v[56:59], v[152:155], v[104:107], v[56:59]\n"
        "s_add_u32 m0, s55, s69\n"
        "v_exp_f32 v4, v4\n"
        "v_mfma_f32_16x16x32_bf16 v[60:63], a[252:255], v[96:99], v[60:63]\n"
        "buffer_load_dwordx4 v117, s[36:39], s54 offen nt lds\n"
        "v_mfma_f32_16x16x32_bf16 v[60:63], v[140:143], v[100:103], v[60:63]\n"
        "v_exp_f32 v5, v5\n"
        "v_mfma_f32_16x16x32_bf16 v[60:63], v[156:159], v[104:107], v[60:63]\n"
        "s_waitcnt lgkmcnt(0)\n"
        "ds_read_b128 v[104:107], v112 offset:512\n"
        "v_cvt_pk_bf16_f32 v2, v4, v5\n"
        "v_mfma_f32_16x16x32_bf16 v[48:51], v[160:163], v[84:87], v[48:51]\n"
        "v_exp_f32 v6, v6\n"
        "v_mfma_f32_16x16x32_bf16 v[48:51], v[176:179], v[80:83], v[48:51]\n"
        "v_fma_f32 v7, v7, s52, v118\n"
        "v_fma_f32 v8, v8, s52, v118\n"
        "v_mfma_f32_16x16x32_bf16 v[48:51], v[192:195], v[92:95], v[48:51]\n"
        "ds_read_b128 v[100:103], v110 offset:0\n"
        "v_fma_f32 v9, v9, s52, v118\n"
        "v_mfma_f32_16x16x32_bf16 v[52:55], v[164:167], v[84:87], v[52:55]\n"
        "ds_read_b128 v[88:91], v110 offset:1024\n"
        "v_fma_f32 v10, v10, s52, v118\n"
        "v_mfma_f32_16x16x32_bf16 v[52:55], v[180:183], v[80:83], v[52:55]\n"
        "ds_read_b128 v[96:99], v110 offset:2048\n"
        "v_add_u32 v110, s60, v110\n"
        "v_mfma_f32_16x16x32_bf16 v[52:55], v[196:199], v[92:95], v[52:55]\n"
        "s_waitcnt lgkmcnt(3)\n"
        "v_exp_f32 v7, v7\n"
        "v_mfma_f32_16x16x32_bf16 v[56:59], v[168:171], v[84:87], v[56:59]\n"
        "v_add_f32 v120, v104, v105\n"
        "v_add_f32 v106, v106, v107\n"
        "v_mfma_f32_16x16x32_bf16 v[56:59], v[184:187], v[80:83], v[56:59]\n"
        "v_add_f32 v120, v120, v106\n"
        "v_cvt_pk_bf16_f32 v3, v6, v7\n"
        "v_mfma_f32_16x16x32_bf16 v[56:59], v[200:203], v[92:95], v[56:59]\n"
        "v_rcp_f32 v120, v120\n"
        "v_mfma_f32_16x16x32_bf16 v[60:63], v[172:175], v[84:87], v[60:63]\n"
        "v_exp_f32 v8, v8\n"
        "v_mfma_f32_16x16x32_bf16 v[60:63], v[188:191], v[80:83], v[60:63]\n"
        "v_exp_f32 v9, v9\n"
        "v_mfma_f32_16x16x32_bf16 v[60:63], v[204:207], v[92:95], v[60:63]\n"
        "s_waitcnt lgkmcnt(0)\n"
        "ds_read_b128 v[92:95], v112 offset:768\n"
        "v_cvt_pk_bf16_f32 v8, v8, v9\n"
        "v_mfma_f32_16x16x32_bf16 v[48:51], v[208:211], v[100:103], v[48:51]\n"
        "v_exp_f32 v10, v10\n"
        "v_mfma_f32_16x16x32_bf16 v[48:51], v[224:227], v[88:91], v[48:51]\n"
        "v_fma_f32 v11, v11, s52, v118\n"
        "v_fma_f32 v12, v12, s52, v118\n"
        "v_mfma_f32_16x16x32_bf16 v[48:51], v[240:243], v[96:99], v[48:51]\n"
        "ds_read_b128 v[80:83], v110 offset:3072\n"
        "v_fma_f32 v13, v13, s52, v118\n"
        "v_mfma_f32_16x16x32_bf16 v[52:55], v[212:215], v[100:103], v[52:55]\n"
        "ds_read_b128 v[104:107], v110 offset:4096\n"
        "v_fma_f32 v14, v14, s52, v118\n"
        "v_mfma_f32_16x16x32_bf16 v[52:55], v[228:231], v[88:91], v[52:55]\n"
        "ds_read_b128 v[84:87], v110 offset:5120\n"
        "v_add_u32 v110, s58, v110\n"
        "v_mfma_f32_16x16x32_bf16 v[52:55], v[244:247], v[96:99], v[52:55]\n"
        "s_waitcnt lgkmcnt(3)\n"
        "v_exp_f32 v11, v11\n"
        "v_mfma_f32_16x16x32_bf16 v[56:59], v[216:219], v[100:103], v[56:59]\n"
        "ds_write_b128 v109, v[0:3] offset:0\n"
        "v_mfma_f32_16x16x32_bf16 v[56:59], v[232:235], v[88:91], v[56:59]\n"
        "v_add_f32 v121, v92, v93\n"
        "v_add_f32 v94, v94, v95\n"
        "v_mfma_f32_16x16x32_bf16 v[56:59], v[248:251], v[96:99], v[56:59]\n"
        "v_add_f32 v121, v121, v94\n"
        "v_cvt_pk_bf16_f32 v9, v10, v11\n"
        "v_mfma_f32_16x16x32_bf16 v[60:63], v[220:223], v[100:103], v[60:63]\n"
        "v_rcp_f32 v121, v121\n"
        "v_mfma_f32_16x16x32_bf16 v[60:63], v[236:239], v[88:91], v[60:63]\n"
        "v_exp_f32 v12, v12\n"
        "v_mfma_f32_16x16x32_bf16 v[60:63], v[252:255], v[96:99], v[60:63]\n"
        "s_waitcnt lgkmcnt(1)\n"
        "v_exp_f32 v13, v13\n"
        "v_mfma_f32_16x16x32_bf16 v[64:67], a[240:243], v[80:83], v[64:67]\n"
        "v_cvt_pk_bf16_f32 v10, v12, v13\n"
        "v_fma_f32 v15, v15, s52, v118\n"
        "v_mfma_f32_16x16x32_bf16 v[64:67], v[128:131], v[104:107], v[64:67]\n"
        "v_exp_f32 v14, v14\n"
        "v_mfma_f32_16x16x32_bf16 v[64:67], v[144:147], v[84:87], v[64:67]\n"
        "ds_read_b128 v[96:99], v110 offset:3072\n"
        "v_fma_f32 v16, v16, s52, v118\n"
        "v_mfma_f32_16x16x32_bf16 v[68:71], a[244:247], v[80:83], v[68:71]\n"
        "ds_read_b128 v[88:91], v110 offset:4096\n"
        "v_fma_f32 v17, v17, s52, v118\n"
        "v_mfma_f32_16x16x32_bf16 v[68:71], v[132:135], v[104:107], v[68:71]\n"
        "ds_read_b128 v[92:95], v110 offset:5120\n"
        "v_add_u32 v110, s59, v110\n"
        "v_mfma_f32_16x16x32_bf16 v[68:71], v[148:151], v[84:87], v[68:71]\n"
        "v_exp_f32 v15, v15\n"
        "v_mfma_f32_16x16x32_bf16 v[72:75], a[248:251], v[80:83], v[72:75]\n"
        "v_cvt_pk_bf16_f32 v11, v14, v15\n"
        "v_fma_f32 v18, v18, s52, v118\n"
        "v_mfma_f32_16x16x32_bf16 v[72:75], v[136:139], v[104:107], v[72:75]\n"
        "ds_write_b128 v109, v[8:11] offset:1024\n"
        "v_mfma_f32_16x16x32_bf16 v[72:75], v[152:155], v[84:87], v[72:75]\n"
        "v_exp_f32 v16, v16\n"
        "v_mfma_f32_16x16x32_bf16 v[76:79], a[252:255], v[80:83], v[76:79]\n"
        "s_add_u32 m0, m0, 528\n"
        "v_exp_f32 v17, v17\n"
        "v_mfma_f32_16x16x32_bf16 v[76:79], v[140:143], v[104:107], v[76:79]\n"
        "buffer_load_dwordx4 v117, s[36:39], s54 offen offset:512 nt lds\n"
        "v_mfma_f32_16x16x32_bf16 v[76:79], v[156:159], v[84:87], v[76:79]\n"
        "s_waitcnt lgkmcnt(1)\n"
        "v_cvt_pk_bf16_f32 v16, v16, v17\n"
        "v_fma_f32 v19, v19, s52, v118\n"
        "v_mfma_f32_16x16x32_bf16 v[64:67], v[160:163], v[96:99], v[64:67]\n"
        "v_exp_f32 v18, v18\n"
        "v_mfma_f32_16x16x32_bf16 v[64:67], v[176:179], v[88:91], v[64:67]\n"
        "v_exp_f32 v19, v19\n"
        "v_mfma_f32_16x16x32_bf16 v[64:67], v[192:195], v[92:95], v[64:67]\n"
        "s_mov_b32 s64, s61\n"
        "ds_read_b128 v[84:87], v110 offset:3072\n"
        "v_cvt_pk_bf16_f32 v17, v18, v19\n"
        "v_mfma_f32_16x16x32_bf16 v[68:71], v[164:167], v[96:99], v[68:71]\n"
        "s_add_u32 s61, s61, 16640\n"
        "ds_read_b128 v[104:107], v110 offset:4096\n"
        "v_fma_f32 v20, v20, s52, v118\n"
        "v_mfma_f32_16x16x32_bf16 v[68:71], v[180:183], v[88:91], v[68:71]\n"
        "s_cmp_eq_u32 s61, 108288\n"
        "s_cselect_b32 s61, 58368, s61\n"
        "ds_read_b128 v[80:83], v110 offset:5120\n"
        "v_fma_f32 v24, v24, s52, v119\n"
        "v_mfma_f32_16x16x32_bf16 v[68:71], v[196:199], v[92:95], v[68:71]\n"
        "s_sub_u32 s62, s61, s64\n"
        "v_exp_f32 v20, v20\n"
        "v_mfma_f32_16x16x32_bf16 v[72:75], v[168:171], v[96:99], v[72:75]\n"
        "v_fma_f32 v21, v21, s52, v118\n"
        "v_fma_f32 v25, v25, s52, v119\n"
        "v_mfma_f32_16x16x32_bf16 v[72:75], v[184:187], v[88:91], v[72:75]\n"
        "v_exp_f32 v24, v24\n"
        "v_mfma_f32_16x16x32_bf16 v[72:75], v[200:203], v[92:95], v[72:75]\n"
        "v_exp_f32 v21, v21\n"
        "v_mfma_f32_16x16x32_bf16 v[76:79], v[172:175], v[96:99], v[76:79]\n"
        "v_cvt_pk_bf16_f32 v18, v20, v21\n"
        "v_fma_f32 v22, v22, s52, v118\n"
        "v_mfma_f32_16x16x32_bf16 v[76:79], v[188:191], v[88:91], v[76:79]\n"
        "v_exp_f32 v25, v25\n"
        "v_mfma_f32_16x16x32_bf16 v[76:79], v[204:207], v[92:95], v[76:79]\n"
        "s_waitcnt lgkmcnt(0)\n"
        "v_cvt_pk_bf16_f32 v24, v24, v25\n"
        "v_fma_f32 v26, v26, s52, v119\n"
        "v_mfma_f32_16x16x32_bf16 v[64:67], v[208:211], v[84:87], v[64:67]\n"
        "v_exp_f32 v22, v22\n"
        "v_mfma_f32_16x16x32_bf16 v[64:67], v[224:227], v[104:107], v[64:67]\n"
        "v_fma_f32 v23, v23, s52, v118\n"
        "v_fma_f32 v27, v27, s52, v119\n"
        "v_mfma_f32_16x16x32_bf16 v[64:67], v[240:243], v[80:83], v[64:67]\n"
        "v_exp_f32 v26, v26\n"
        "v_mfma_f32_16x16x32_bf16 v[68:71], v[212:215], v[84:87], v[68:71]\n"
        "v_exp_f32 v23, v23\n"
        "v_mfma_f32_16x16x32_bf16 v[68:71], v[228:231], v[104:107], v[68:71]\n"
        "v_cvt_pk_bf16_f32 v19, v22, v23\n"
        "v_fma_f32 v28, v28, s52, v119\n"
        "v_mfma_f32_16x16x32_bf16 v[68:71], v[244:247], v[80:83], v[68:71]\n"
        "ds_read_b128 v[92:95], v108 offset:0\n"
        "v_fma_f32 v29, v29, s52, v119\n"
        "v_mfma_f32_16x16x32_bf16 v[72:75], v[216:219], v[84:87], v[72:75]\n"
        "ds_read_b128 v[88:91], v108 offset:32\n"
        "v_fma_f32 v30, v30, s52, v119\n"
        "v_mfma_f32_16x16x32_bf16 v[72:75], v[232:235], v[104:107], v[72:75]\n"
        "ds_read_b128 v[96:99], v108 offset:64\n"
        "v_fma_f32 v31, v31, s52, v119\n"
        "v_mfma_f32_16x16x32_bf16 v[72:75], v[248:251], v[80:83], v[72:75]\n"
        "ds_read_b128 v[100:103], v108 offset:96\n"
        "v_fma_f32 v32, v32, s52, v119\n"
        "v_mfma_f32_16x16x32_bf16 v[76:79], v[220:223], v[84:87], v[76:79]\n"
        "v_exp_f32 v27, v27\n"
        "v_mfma_f32_16x16x32_bf16 v[76:79], v[236:239], v[104:107], v[76:79]\n"
        "v_cvt_pk_bf16_f32 v25, v26, v27\n"
        "v_fma_f32 v33, v33, s52, v119\n"
        "v_mfma_f32_16x16x32_bf16 v[76:79], v[252:255], v[80:83], v[76:79]\n"
        "v_exp_f32 v28, v28\n"
        "v_mfma_f32_16x16x32_bf16 v[80:83], v[124:127], v[0:3], 0\n"
        "v_exp_f32 v29, v29\n"
        "v_mfma_f32_16x16x32_bf16 v[80:83], v[124:127], v[8:11], v[80:83]\n"
        "v_cvt_pk_bf16_f32 v26, v28, v29\n"
        "v_fma_f32 v34, v34, s52, v119\n"
        "v_mfma_f32_16x16x32_bf16 v[80:83], v[124:127], v[16:19], v[80:83]\n"
        "s_waitcnt lgkmcnt(0)\n"
        "v_exp_f32 v30, v30\n"
        "v_mfma_f32_16x16x32_bf16 v[0:3], a[0:3], v[92:95], 0\n"
        "v_exp_f32 v31, v31\n"
        "v_mfma_f32_16x16x32_bf16 v[0:3], a[4:7], v[88:91], v[0:3]\n"
        "v_cvt_pk_bf16_f32 v27, v30, v31\n"
        "v_fma_f32 v35, v35, s52, v119\n"
        "v_mfma_f32_16x16x32_bf16 v[0:3], a[8:11], v[96:99], v[0:3]\n"
        "ds_write_b32 v113, v80 offset:512\n"
        "v_mfma_f32_16x16x32_bf16 v[0:3], a[12:15], v[100:103], v[0:3]\n"
        "ds_write_b128 v109, v[24:27] offset:3072\n"
        "v_mfma_f32_16x16x32_bf16 v[4:7], a[32:35], v[92:95], 0\n"
        "v_exp_f32 v32, v32\n"
        "v_mfma_f32_16x16x32_bf16 v[4:7], a[36:39], v[88:91], v[4:7]\n"
        "v_exp_f32 v33, v33\n"
        "v_mfma_f32_16x16x32_bf16 v[4:7], a[40:43], v[96:99], v[4:7]\n"
        "v_cvt_pk_bf16_f32 v32, v32, v33\n"
        "v_fma_f32 v36, v36, s52, v119\n"
        "v_mfma_f32_16x16x32_bf16 v[4:7], a[44:47], v[100:103], v[4:7]\n"
        "s_add_u32 m0, m0, 528\n"
        "v_exp_f32 v34, v34\n"
        "v_mfma_f32_16x16x32_bf16 v[8:11], a[64:67], v[92:95], 0\n"
        "buffer_load_dwordx4 v117, s[36:39], s54 offen offset:1024 nt lds\n"
        "v_mfma_f32_16x16x32_bf16 v[8:11], a[68:71], v[88:91], v[8:11]\n"
        "ds_write_b128 v109, v[16:19] offset:2048\n"
        "v_mfma_f32_16x16x32_bf16 v[8:11], a[72:75], v[96:99], v[8:11]\n"
        "v_exp_f32 v35, v35\n"
        "v_mfma_f32_16x16x32_bf16 v[8:11], a[76:79], v[100:103], v[8:11]\n"
        "v_cvt_pk_bf16_f32 v33, v34, v35\n"
        "v_fma_f32 v37, v37, s52, v119\n"
        "v_mfma_f32_16x16x32_bf16 v[12:15], a[96:99], v[92:95], 0\n"
        "v_exp_f32 v36, v36\n"
        "v_mfma_f32_16x16x32_bf16 v[12:15], a[100:103], v[88:91], v[12:15]\n"
        "v_exp_f32 v37, v37\n"
        "v_mfma_f32_16x16x32_bf16 v[12:15], a[104:107], v[96:99], v[12:15]\n"
        "ds_read_b128 v[80:83], v108 offset:128\n"
        "v_cvt_pk_bf16_f32 v34, v36, v37\n"
        "v_mfma_f32_16x16x32_bf16 v[12:15], a[108:111], v[100:103], v[12:15]\n"
        "ds_read_b128 v[104:107], v108 offset:160\n"
        "v_fma_f32 v38, v38, s52, v119\n"
        "v_mfma_f32_16x16x32_bf16 v[16:19], a[128:131], v[92:95], 0\n"
        "ds_read_b128 v[84:87], v108 offset:192\n"
        "v_fma_f32 v39, v39, s52, v119\n"
        "v_mfma_f32_16x16x32_bf16 v[16:19], a[132:135], v[88:91], v[16:19]\n"
        "v_exp_f32 v38, v38\n"
        "v_mfma_f32_16x16x32_bf16 v[16:19], a[136:139], v[96:99], v[16:19]\n"
        "v_exp_f32 v39, v39\n"
        "v_mfma_f32_16x16x32_bf16 v[16:19], a[140:143], v[100:103], v[16:19]\n"
        "v_cvt_pk_bf16_f32 v35, v38, v39\n"
        "v_fma_f32 v40, v40, s52, v119\n"
        "v_mfma_f32_16x16x32_bf16 v[20:23], a[160:163], v[92:95], 0\n"
        "ds_read_b128 v[92:95], v108 offset:224\n"
        "v_fma_f32 v41, v41, s52, v119\n"
        "v_mfma_f32_16x16x32_bf16 v[20:23], a[164:167], v[88:91], v[20:23]\n"
        "ds_write_b128 v109, v[32:35] offset:4096\n"
        "v_mfma_f32_16x16x32_bf16 v[20:23], a[168:171], v[96:99], v[20:23]\n"
        "v_exp_f32 v40, v40\n"
        "v_mfma_f32_16x16x32_bf16 v[20:23], a[172:175], v[100:103], v[20:23]\n"
        "s_waitcnt lgkmcnt(2)\n"
        "v_exp_f32 v41, v41\n"
        "v_mfma_f32_16x16x32_bf16 v[0:3], a[16:19], v[80:83], v[0:3]\n"
        "v_cvt_pk_bf16_f32 v40, v40, v41\n"
        "v_fma_f32 v42, v42, s52, v119\n"
        "v_mfma_f32_16x16x32_bf16 v[0:3], a[20:23], v[104:107], v[0:3]\n"
        "s_waitcnt lgkmcnt(1)\n"
        "v_exp_f32 v42, v42\n"
        "v_mfma_f32_16x16x32_bf16 v[0:3], a[24:27], v[84:87], v[0:3]\n"
        "v_fma_f32 v43, v43, s52, v119\n"
        "v_fma_f32 v44, v44, s52, v119\n"
        "v_mfma_f32_16x16x32_bf16 v[0:3], a[28:31], v[92:95], v[0:3]\n"
        "v_exp_f32 v43, v43\n"
        "v_mfma_f32_16x16x32_bf16 v[4:7], a[48:51], v[80:83], v[4:7]\n"
        "v_cvt_pk_bf16_f32 v41, v42, v43\n"
        "v_fma_f32 v45, v45, s52, v119\n"
        "v_mfma_f32_16x16x32_bf16 v[4:7], a[52:55], v[104:107], v[4:7]\n"
        "v_exp_f32 v44, v44\n"
        "v_mfma_f32_16x16x32_bf16 v[4:7], a[56:59], v[84:87], v[4:7]\n"
        "v_exp_f32 v45, v45\n"
        "v_mfma_f32_16x16x32_bf16 v[4:7], a[60:63], v[92:95], v[4:7]\n"
        "v_cvt_pk_bf16_f32 v42, v44, v45\n"
        "v_fma_f32 v46, v46, s52, v119\n"
        "v_mfma_f32_16x16x32_bf16 v[8:11], a[80:83], v[80:83], v[8:11]\n"
        "v_exp_f32 v46, v46\n"
        "v_mfma_f32_16x16x32_bf16 v[8:11], a[84:87], v[104:107], v[8:11]\n"
        "v_fma_f32 v47, v47, s52, v119\n"
        "v_mul_f32 v48, v120, v48\n"
        "v_mfma_f32_16x16x32_bf16 v[8:11], a[88:91], v[84:87], v[8:11]\n"
        "v_exp_f32 v47, v47\n"
        "v_mfma_f32_16x16x32_bf16 v[8:11], a[92:95], v[92:95], v[8:11]\n"
        "v_cvt_pk_bf16_f32 v43, v46, v47\n"
        "v_mul_f32 v49, v120, v49\n"
        "v_mfma_f32_16x16x32_bf16 v[12:15], a[112:115], v[80:83], v[12:15]\n"
        "ds_write_b128 v109, v[40:43] offset:5120\n"
        "v_mfma_f32_16x16x32_bf16 v[12:15], a[116:119], v[104:107], v[12:15]\n"
        "v_mul_f32 v50, v120, v50\n"
        "v_mul_f32 v51, v120, v51\n"
        "v_mfma_f32_16x16x32_bf16 v[12:15], a[120:123], v[84:87], v[12:15]\n"
        "v_mul_f32 v64, v121, v64\n"
        "v_cvt_pk_bf16_f32 v48, v48, v49\n"
        "v_mfma_f32_16x16x32_bf16 v[12:15], a[124:127], v[92:95], v[12:15]\n"
        "v_mul_f32 v65, v121, v65\n"
        "v_mfma_f32_16x16x32_bf16 v[16:19], a[144:147], v[80:83], v[16:19]\n"
        "v_cvt_pk_bf16_f32 v49, v50, v51\n"
        "v_mul_f32 v66, v121, v66\n"
        "v_mfma_f32_16x16x32_bf16 v[16:19], a[148:151], v[104:107], v[16:19]\n"
        "ds_read_b128 v[88:91], v108 offset:512\n"
        "v_mul_f32 v67, v121, v67\n"
        "v_mfma_f32_16x16x32_bf16 v[16:19], a[152:155], v[84:87], v[16:19]\n"
        "ds_read_b128 v[100:103], v108 offset:544\n"
        "v_cvt_pk_bf16_f32 v64, v64, v65\n"
        "v_mfma_f32_16x16x32_bf16 v[16:19], a[156:159], v[92:95], v[16:19]\n"
        "ds_read_b128 v[96:99], v108 offset:576\n"
        "v_cvt_pk_bf16_f32 v65, v66, v67\n"
        "v_mfma_f32_16x16x32_bf16 v[20:23], a[176:179], v[80:83], v[20:23]\n"
        "s_add_u32 m0, m0, 528\n"
        "v_mul_f32 v52, v120, v52\n"
        "v_mul_f32 v53, v120, v53\n"
        "v_mfma_f32_16x16x32_bf16 v[20:23], a[180:183], v[104:107], v[20:23]\n"
        "ds_read_b128 v[104:107], v108 offset:608\n"
        "v_mul_f32 v54, v120, v54\n"
        "v_mfma_f32_16x16x32_bf16 v[20:23], a[184:187], v[84:87], v[20:23]\n"
        "buffer_load_dwordx4 v117, s[36:39], s54 offen offset:1536 nt lds\n"
        "v_mfma_f32_16x16x32_bf16 v[20:23], a[188:191], v[92:95], v[20:23]\n"
        "s_add_u32 s54, s54, 16384\n"
        "ds_write_b64 v114, v[48:49] offset:0\n"
        "v_mfma_f32_16x16x32_bf16 v[92:95], v[124:127], v[24:27], 0\n"
        "s_mov_b32 s55, s64\n"
        "v_mul_f32 v55, v120, v55\n"
        "v_mul_f32 v68, v121, v68\n"
        "v_mfma_f32_16x16x32_bf16 v[92:95], v[124:127], v[32:35], v[92:95]\n"
        "ds_write_b64 v114, v[64:65] offset:2304\n"
        "v_mfma_f32_16x16x32_bf16 v[92:95], v[124:127], v[40:43], v[92:95]\n"
        "s_waitcnt lgkmcnt(2)\n"
        "v_cvt_pk_bf16_f32 v52, v52, v53\n"
        "v_mul_f32 v69, v121, v69\n"
        "v_mfma_f32_16x16x32_bf16 v[24:27], a[0:3], v[88:91], 0\n"
        "v_cvt_pk_bf16_f32 v53, v54, v55\n"
        "v_mul_f32 v70, v121, v70\n"
        "v_mfma_f32_16x16x32_bf16 v[24:27], a[4:7], v[100:103], v[24:27]\n"
        "ds_write_b64 v114, v[52:53] offset:32\n"
        "v_mfma_f32_16x16x32_bf16 v[24:27], a[8:11], v[96:99], v[24:27]\n"
        "v_mul_f32 v71, v121, v71\n"
        "v_cvt_pk_bf16_f32 v68, v68, v69\n"
        "v_mfma_f32_16x16x32_bf16 v[24:27], a[12:15], v[104:107], v[24:27]\n"
        "ds_write_b32 v113, v92 offset:768\n"
        "v_mfma_f32_16x16x32_bf16 v[28:31], a[32:35], v[88:91], 0\n"
        "v_cvt_pk_bf16_f32 v69, v70, v71\n"
        "v_mul_f32 v56, v120, v56\n"
        "v_mfma_f32_16x16x32_bf16 v[28:31], a[36:39], v[100:103], v[28:31]\n"
        "v_mul_f32 v57, v120, v57\n"
        "v_max_f32 v118, v0, v1\n"
        "v_mfma_f32_16x16x32_bf16 v[28:31], a[40:43], v[96:99], v[28:31]\n"
        "ds_write_b64 v114, v[68:69] offset:2336\n"
        "v_mfma_f32_16x16x32_bf16 v[28:31], a[44:47], v[104:107], v[28:31]\n"
        "v_mul_f32 v58, v120, v58\n"
        "v_max3_f32 v118, v118, v2, v3\n"
        "v_mfma_f32_16x16x32_bf16 v[32:35], a[64:67], v[88:91], 0\n"
        "v_mul_f32 v59, v120, v59\n"
        "v_max3_f32 v118, v118, v4, v5\n"
        "v_mfma_f32_16x16x32_bf16 v[32:35], a[68:71], v[100:103], v[32:35]\n"
        "v_mul_f32 v72, v121, v72\n"
        "v_mfma_f32_16x16x32_bf16 v[32:35], a[72:75], v[96:99], v[32:35]\n"
        "v_cvt_pk_bf16_f32 v56, v56, v57\n"
        "v_max3_f32 v118, v118, v6, v7\n"
        "v_mfma_f32_16x16x32_bf16 v[32:35], a[76:79], v[104:107], v[32:35]\n"
        "v_mul_f32 v73, v121, v73\n"
        "v_cvt_pk_bf16_f32 v57, v58, v59\n"
        "v_mfma_f32_16x16x32_bf16 v[36:39], a[96:99], v[88:91], 0\n"
        "ds_write_b64 v114, v[56:57] offset:64\n"
        "v_mfma_f32_16x16x32_bf16 v[36:39], a[100:103], v[100:103], v[36:39]\n"
        "v_max3_f32 v118, v118, v8, v9\n"
        "v_mul_f32 v74, v121, v74\n"
        "v_mfma_f32_16x16x32_bf16 v[36:39], a[104:107], v[96:99], v[36:39]\n"
        "ds_read_b128 v[92:95], v108 offset:640\n"
        "v_max3_f32 v118, v118, v10, v11\n"
        "v_mfma_f32_16x16x32_bf16 v[36:39], a[108:111], v[104:107], v[36:39]\n"
        "ds_read_b128 v[84:87], v108 offset:672\n"
        "v_mul_f32 v75, v121, v75\n"
        "v_mfma_f32_16x16x32_bf16 v[40:43], a[128:131], v[88:91], 0\n"
        "ds_read_b128 v[80:83], v108 offset:704\n"
        "v_max3_f32 v118, v118, v12, v13\n"
        "v_mfma_f32_16x16x32_bf16 v[40:43], a[132:135], v[100:103], v[40:43]\n"
        "v_cvt_pk_bf16_f32 v72, v72, v73\n"
        "v_max3_f32 v118, v118, v14, v15\n"
        "v_mfma_f32_16x16x32_bf16 v[40:43], a[136:139], v[96:99], v[40:43]\n"
        "v_mul_f32 v60, v120, v60\n"
        "v_mul_f32 v61, v120, v61\n"
        "v_mfma_f32_16x16x32_bf16 v[40:43], a[140:143], v[104:107], v[40:43]\n"
        "v_mul_f32 v62, v120, v62\n"
        "v_mul_f32 v63, v120, v63\n"
        "v_mfma_f32_16x16x32_bf16 v[44:47], a[160:163], v[88:91], 0\n"
        "ds_read_b128 v[88:91], v108 offset:736\n"
        "v_cvt_pk_bf16_f32 v60, v60, v61\n"
        "v_mfma_f32_16x16x32_bf16 v[44:47], a[164:167], v[100:103], v[44:47]\n"
        "v_cvt_pk_bf16_f32 v61, v62, v63\n"
        "v_cvt_pk_bf16_f32 v73, v74, v75\n"
        "v_mfma_f32_16x16x32_bf16 v[44:47], a[168:171], v[96:99], v[44:47]\n"
        "v_max3_f32 v118, v118, v16, v17\n"
        "v_max3_f32 v118, v118, v18, v19\n"
        "v_mfma_f32_16x16x32_bf16 v[44:47], a[172:175], v[104:107], v[44:47]\n"
        "s_waitcnt lgkmcnt(1)\n"
        "ds_write_b64 v114, v[60:61] offset:96\n"
        "v_mfma_f32_16x16x32_bf16 v[24:27], a[16:19], v[92:95], v[24:27]\n"
        "v_max3_f32 v118, v118, v20, v21\n"
        "v_max3_f32 v118, v118, v22, v23\n"
        "v_mfma_f32_16x16x32_bf16 v[24:27], a[20:23], v[84:87], v[24:27]\n"
        "s_waitcnt lgkmcnt(1)\n"
        "ds_read_b128 v[48:51], v115 offset:0\n"
        "v_mul_f32 v76, v121, v76\n"
        "v_mfma_f32_16x16x32_bf16 v[24:27], a[24:27], v[80:83], v[24:27]\n"
        "ds_write_b64 v114, v[72:73] offset:2368\n"
        "v_mfma_f32_16x16x32_bf16 v[24:27], a[28:31], v[88:91], v[24:27]\n"
        "v_mul_f32 v77, v121, v77\n"
        "v_mul_f32 v78, v121, v78\n"
        "v_mfma_f32_16x16x32_bf16 v[28:31], a[48:51], v[92:95], v[28:31]\n"
        "v_mul_f32 v79, v121, v79\n"
        "v_cvt_pk_bf16_f32 v76, v76, v77\n"
        "v_mfma_f32_16x16x32_bf16 v[28:31], a[52:55], v[84:87], v[28:31]\n"
        "ds_read_b128 v[52:55], v115 offset:1152\n"
        "v_cvt_pk_bf16_f32 v77, v78, v79\n"
        "v_mfma_f32_16x16x32_bf16 v[28:31], a[56:59], v[80:83], v[28:31]\n"
        "s_waitcnt lgkmcnt(2)\n"
        "ds_write_b64 v114, v[76:77] offset:2400\n"
        "v_mfma_f32_16x16x32_bf16 v[28:31], a[60:63], v[88:91], v[28:31]\n"
        "buffer_store_dwordx4 v[48:51], v116, s[48:51], s56 offen sc1\n"
        "v_mfma_f32_16x16x32_bf16 v[32:35], a[80:83], v[92:95], v[32:35]\n"
        "s_add_u32 s56, s56, 4096\n"
        "ds_read_b128 v[64:67], v115 offset:2304\n"
        "ds_read_b128 v[68:71], v115 offset:3456\n"
        "v_mfma_f32_16x16x32_bf16 v[32:35], a[84:87], v[84:87], v[32:35]\n"
        "v_max_f32 v119, v24, v25\n"
        "v_max3_f32 v119, v119, v26, v27\n"
        "v_mfma_f32_16x16x32_bf16 v[32:35], a[88:91], v[80:83], v[32:35]\n"
        "s_waitcnt lgkmcnt(3)\n"
        "buffer_store_dwordx4 v[52:55], v116, s[48:51], s56 offen sc1\n"
        "v_mfma_f32_16x16x32_bf16 v[32:35], a[92:95], v[88:91], v[32:35]\n"
        "s_add_u32 s56, s56, 4096\n"
        "v_max3_f32 v119, v119, v28, v29\n"
        "v_max3_f32 v119, v119, v30, v31\n"
        "v_mfma_f32_16x16x32_bf16 v[36:39], a[112:115], v[92:95], v[36:39]\n"
        "v_add_u32 v108, s62, v108\n"
        "v_add_u32 v110, s57, v109\n"
        "v_mfma_f32_16x16x32_bf16 v[36:39], a[116:119], v[84:87], v[36:39]\n"
        "s_waitcnt lgkmcnt(1)\n"
        "buffer_store_dwordx4 v[64:67], v116, s[48:51], s56 offen sc1\n"
        "v_mfma_f32_16x16x32_bf16 v[36:39], a[120:123], v[80:83], v[36:39]\n"
        "s_waitcnt lgkmcnt(0)\n"
        "ds_read_b128 v[104:107], v109 offset:0\n"
        "v_max3_f32 v119, v119, v32, v33\n"
        "v_mfma_f32_16x16x32_bf16 v[36:39], a[124:127], v[88:91], v[36:39]\n"
        "s_add_u32 s56, s56, 4096\n"
        "ds_read_b128 v[96:99], v109 offset:1024\n"
        "v_max3_f32 v119, v119, v34, v35\n"
        "v_mfma_f32_16x16x32_bf16 v[40:43], a[144:147], v[92:95], v[40:43]\n"
        "ds_read_b128 v[100:103], v109 offset:2048\n"
        "v_xor_b32 v112, 0x8000, v112\n"
        "v_mfma_f32_16x16x32_bf16 v[40:43], a[148:151], v[84:87], v[40:43]\n"
        "buffer_store_dwordx4 v[68:71], v116, s[48:51], s56 offen sc1\n"
        "v_mfma_f32_16x16x32_bf16 v[40:43], a[152:155], v[80:83], v[40:43]\n"
        "s_add_u32 s56, s56, 4096\n"
        "v_max3_f32 v119, v119, v36, v37\n"
        "v_max3_f32 v119, v119, v38, v39\n"
        "v_mfma_f32_16x16x32_bf16 v[40:43], a[156:159], v[88:91], v[40:43]\n"
        "v_xor_b32 v113, 0x8000, v113\n"
        "v_mfma_f32_16x16x32_bf16 v[44:47], a[176:179], v[92:95], v[44:47]\n"
        "v_mfma_f32_16x16x32_bf16 v[44:47], a[180:183], v[84:87], v[44:47]\n"
        "ds_read_b128 v[84:87], v109 offset:4096\n"
        "v_mfma_f32_16x16x32_bf16 v[44:47], a[184:187], v[80:83], v[44:47]\n"
        "ds_read_b128 v[80:83], v109 offset:3072\n"
        "v_max3_f32 v119, v119, v40, v41\n"
        "v_mfma_f32_16x16x32_bf16 v[44:47], a[188:191], v[88:91], v[44:47]\n"
        "s_waitcnt lgkmcnt(2)\n"
        "ds_read_b128 v[88:91], v109 offset:5120\n"
        "v_max3_f32 v119, v119, v42, v43\n"
        "v_mfma_f32_16x16x32_bf16 v[48:51], a[192:195], v[104:107], 0\n"
        "s_branch 1b\n"
        "3:\n"
        "v_mfma_f32_16x16x32_bf16 v[64:67], a[192:195], v[80:83], 0\n"
        "ds_read_b128 v[92:95], v112 offset:0\n"
        "v_mfma_f32_16x16x32_bf16 v[64:67], a[208:211], v[84:87], v[64:67]\n"
        "v_mfma_f32_16x16x32_bf16 v[64:67], a[224:227], v[88:91], v[64:67]\n"
        "ds_read_b128 v[96:99], v110 offset:0\n"
        "v_mfma_f32_16x16x32_bf16 v[68:71], a[196:199], v[80:83], 0\n"
        "ds_read_b128 v[100:103], v110 offset:1024\n"
        "v_mfma_f32_16x16x32_bf16 v[68:71], a[212:215], v[84:87], v[68:71]\n"
        "ds_read_b128 v[104:107], v110 offset:2048\n"
        "v_add_u32 v110, s58, v110\n"
        "v_mfma_f32_16x16x32_bf16 v[68:71], a[228:231], v[88:91], v[68:71]\n"
        "s_waitcnt lgkmcnt(3)\n"
        "v_mfma_f32_16x16x32_bf16 v[72:75], a[200:203], v[80:83], 0\n"
        "v_max3_f32 v118, v92, v93, v94\n"
        "v_max_f32 v118, v118, v95\n"
        "v_mfma_f32_16x16x32_bf16 v[72:75], a[216:219], v[84:87], v[72:75]\n"
        "v_mul_f32 v118, -s52, v118\n"
        "v_fma_f32 v0, v0, s52, v118\n"
        "v_mfma_f32_16x16x32_bf16 v[72:75], a[232:235], v[88:91], v[72:75]\n"
        "v_exp_f32 v0, v0\n"
        "v_mfma_f32_16x16x32_bf16 v[76:79], a[204:207], v[80:83], 0\n"
        "v_fma_f32 v1, v1, s52, v118\n"
        "v_fma_f32 v2, v2, s52, v118\n"
        "v_mfma_f32_16x16x32_bf16 v[76:79], a[220:223], v[84:87], v[76:79]\n"
        "v_exp_f32 v1, v1\n"
        "v_mfma_f32_16x16x32_bf16 v[76:79], a[236:239], v[88:91], v[76:79]\n"
        "s_waitcnt lgkmcnt(0)\n"
        "ds_read_b128 v[88:91], v112 offset:256\n"
        "v_cvt_pk_bf16_f32 v0, v0, v1\n"
        "v_mfma_f32_16x16x32_bf16 v[48:51], a[240:243], v[96:99], v[48:51]\n"
        "v_exp_f32 v2, v2\n"
        "v_mfma_f32_16x16x32_bf16 v[48:51], v[128:131], v[100:103], v[48:51]\n"
        "v_fma_f32 v3, v3, s52, v118\n"
        "v_fma_f32 v4, v4, s52, v118\n"
        "v_mfma_f32_16x16x32_bf16 v[48:51], v[144:147], v[104:107], v[48:51]\n"
        "ds_read_b128 v[84:87], v110 offset:0\n"
        "v_fma_f32 v5, v5, s52, v118\n"
        "v_mfma_f32_16x16x32_bf16 v[52:55], a[244:247], v[96:99], v[52:55]\n"
        "ds_read_b128 v[80:83], v110 offset:1024\n"
        "v_fma_f32 v6, v6, s52, v118\n"
        "v_mfma_f32_16x16x32_bf16 v[52:55], v[132:135], v[100:103], v[52:55]\n"
        "ds_read_b128 v[92:95], v110 offset:2048\n"
        "v_add_u32 v110, s59, v110\n"
        "v_mfma_f32_16x16x32_bf16 v[52:55], v[148:151], v[104:107], v[52:55]\n"
        "s_waitcnt lgkmcnt(3)\n"
        "v_exp_f32 v3, v3\n"
        "v_mfma_f32_16x16x32_bf16 v[56:59], a[248:251], v[96:99], v[56:59]\n"
        "v_max3_f32 v119, v88, v89, v90\n"
        "v_max_f32 v119, v119, v91\n"
        "v_mfma_f32_16x16x32_bf16 v[56:59], v[136:139], v[100:103], v[56:59]\n"
        "v_mul_f32 v119, -s52, v119\n"
        "v_cvt_pk_bf16_f32 v1, v2, v3\n"
        "v_mfma_f32_16x16x32_bf16 v[56:59], v[152:155], v[104:107], v[56:59]\n"
        "v_exp_f32 v4, v4\n"
        "v_mfma_f32_16x16x32_bf16 v[60:63], a[252:255], v[96:99], v[60:63]\n"
        "v_exp_f32 v5, v5\n"
        "v_mfma_f32_16x16x32_bf16 v[60:63], v[140:143], v[100:103], v[60:63]\n"
        "v_cvt_pk_bf16_f32 v2, v4, v5\n"
        "v_fma_f32 v7, v7, s52, v118\n"
        "v_mfma_f32_16x16x32_bf16 v[60:63], v[156:159], v[104:107], v[60:63]\n"
        "s_waitcnt lgkmcnt(0)\n"
        "ds_read_b128 v[104:107], v112 offset:512\n"
        "v_fma_f32 v8, v8, s52, v118\n"
        "v_mfma_f32_16x16x32_bf16 v[48:51], v[160:163], v[84:87], v[48:51]\n"
        "v_exp_f32 v6, v6\n"
        "v_mfma_f32_16x16x32_bf16 v[48:51], v[176:179], v[80:83], v[48:51]\n"
        "v_exp_f32 v7, v7\n"
        "v_mfma_f32_16x16x32_bf16 v[48:51], v[192:195], v[92:95], v[48:51]\n"
        "ds_read_b128 v[100:103], v110 offset:0\n"
        "v_cvt_pk_bf16_f32 v3, v6, v7\n"
        "v_mfma_f32_16x16x32_bf16 v[52:55], v[164:167], v[84:87], v[52:55]\n"
        "ds_read_b128 v[88:91], v110 offset:1024\n"
        "v_fma_f32 v9, v9, s52, v118\n"
        "v_mfma_f32_16x16x32_bf16 v[52:55], v[180:183], v[80:83], v[52:55]\n"
        "ds_read_b128 v[96:99], v110 offset:2048\n"
        "v_add_u32 v110, s60, v110\n"
        "v_mfma_f32_16x16x32_bf16 v[52:55], v[196:199], v[92:95], v[52:55]\n"
        "s_waitcnt lgkmcnt(3)\n"
        "v_exp_f32 v8, v8\n"
        "v_mfma_f32_16x16x32_bf16 v[56:59], v[168:171], v[84:87], v[56:59]\n"
        "v_add_f32 v120, v104, v105\n"
        "v_add_f32 v106, v106, v107\n"
        "v_mfma_f32_16x16x32_bf16 v[56:59], v[184:187], v[80:83], v[56:59]\n"
        "v_add_f32 v120, v120, v106\n"
        "v_fma_f32 v10, v10, s52, v118\n"
        "v_mfma_f32_16x16x32_bf16 v[56:59], v[200:203], v[92:95], v[56:59]\n"
        "v_rcp_f32 v120, v120\n"
        "v_mfma_f32_16x16x32_bf16 v[60:63], v[172:175], v[84:87], v[60:63]\n"
        "v_exp_f32 v9, v9\n"
        "v_mfma_f32_16x16x32_bf16 v[60:63], v[188:191], v[80:83], v[60:63]\n"
        "v_cvt_pk_bf16_f32 v8, v8, v9\n"
        "v_fma_f32 v11, v11, s52, v118\n"
        "v_mfma_f32_16x16x32_bf16 v[60:63], v[204:207], v[92:95], v[60:63]\n"
        "s_waitcnt lgkmcnt(0)\n"
        "ds_read_b128 v[92:95], v112 offset:768\n"
        "v_fma_f32 v12, v12, s52, v118\n"
        "v_mfma_f32_16x16x32_bf16 v[48:51], v[208:211], v[100:103], v[48:51]\n"
        "v_exp_f32 v10, v10\n"
        "v_mfma_f32_16x16x32_bf16 v[48:51], v[224:227], v[88:91], v[48:51]\n"
        "v_exp_f32 v11, v11\n"
        "v_mfma_f32_16x16x32_bf16 v[48:51], v[240:243], v[96:99], v[48:51]\n"
        "ds_read_b128 v[80:83], v110 offset:3072\n"
        "v_cvt_pk_bf16_f32 v9, v10, v11\n"
        "v_mfma_f32_16x16x32_bf16 v[52:55], v[212:215], v[100:103], v[52:55]\n"
        "ds_read_b128 v[104:107], v110 offset:4096\n"
        "v_fma_f32 v13, v13, s52, v118\n"
        "v_mfma_f32_16x16x32_bf16 v[52:55], v[228:231], v[88:91], v[52:55]\n"
        "ds_read_b128 v[84:87], v110 offset:5120\n"
        "v_add_u32 v110, s58, v110\n"
        "v_mfma_f32_16x16x32_bf16 v[52:55], v[244:247], v[96:99], v[52:55]\n"
        "s_waitcnt lgkmcnt(3)\n"
        "v_exp_f32 v12, v12\n"
        "v_mfma_f32_16x16x32_bf16 v[56:59], v[216:219], v[100:103], v[56:59]\n"
        "v_add_f32 v121, v92, v93\n"
        "v_add_f32 v94, v94, v95\n"
        "v_mfma_f32_16x16x32_bf16 v[56:59], v[232:235], v[88:91], v[56:59]\n"
        "v_add_f32 v121, v121, v94\n"
        "v_fma_f32 v14, v14, s52, v118\n"
        "v_mfma_f32_16x16x32_bf16 v[56:59], v[248:251], v[96:99], v[56:59]\n"
        "ds_write_b128 v109, v[0:3] offset:0\n"
        "v_mfma_f32_16x16x32_bf16 v[60:63], v[220:223], v[100:103], v[60:63]\n"
        "v_rcp_f32 v121, v121\n"
        "v_mfma_f32_16x16x32_bf16 v[60:63], v[236:239], v[88:91], v[60:63]\n"
        "v_exp_f32 v13, v13\n"
        "v_mfma_f32_16x16x32_bf16 v[60:63], v[252:255], v[96:99], v[60:63]\n"
        "s_waitcnt lgkmcnt(1)\n"
        "v_cvt_pk_bf16_f32 v10, v12, v13\n"
        "v_fma_f32 v15, v15, s52, v118\n"
        "v_mfma_f32_16x16x32_bf16 v[64:67], a[240:243], v[80:83], v[64:67]\n"
        "v_exp_f32 v14, v14\n"
        "v_mfma_f32_16x16x32_bf16 v[64:67], v[128:131], v[104:107], v[64:67]\n"
        "v_exp_f32 v15, v15\n"
        "v_mfma_f32_16x16x32_bf16 v[64:67], v[144:147], v[84:87], v[64:67]\n"
        "ds_read_b128 v[96:99], v110 offset:3072\n"
        "v_cvt_pk_bf16_f32 v11, v14, v15\n"
        "v_mfma_f32_16x16x32_bf16 v[68:71], a[244:247], v[80:83], v[68:71]\n"
        "ds_read_b128 v[88:91], v110 offset:4096\n"
        "v_fma_f32 v16, v16, s52, v118\n"
        "v_mfma_f32_16x16x32_bf16 v[68:71], v[132:135], v[104:107], v[68:71]\n"
        "ds_read_b128 v[92:95], v110 offset:5120\n"
        "v_add_u32 v110, s59, v110\n"
        "v_mfma_f32_16x16x32_bf16 v[68:71], v[148:151], v[84:87], v[68:71]\n"
        "v_exp_f32 v16, v16\n"
        "v_mfma_f32_16x16x32_bf16 v[72:75], a[248:251], v[80:83], v[72:75]\n"
        "ds_write_b128 v109, v[8:11] offset:1024\n"
        "v_mfma_f32_16x16x32_bf16 v[72:75], v[136:139], v[104:107], v[72:75]\n"
        "v_fma_f32 v17, v17, s52, v118\n"
        "v_fma_f32 v18, v18, s52, v118\n"
        "v_mfma_f32_16x16x32_bf16 v[72:75], v[152:155], v[84:87], v[72:75]\n"
        "v_exp_f32 v17, v17\n"
        "v_mfma_f32_16x16x32_bf16 v[76:79], a[252:255], v[80:83], v[76:79]\n"
        "v_cvt_pk_bf16_f32 v16, v16, v17\n"
        "v_fma_f32 v24, v24, s52, v119\n"
        "v_mfma_f32_16x16x32_bf16 v[76:79], v[140:143], v[104:107], v[76:79]\n"
        "v_exp_f32 v18, v18\n"
        "v_mfma_f32_16x16x32_bf16 v[76:79], v[156:159], v[84:87], v[76:79]\n"
        "s_waitcnt lgkmcnt(1)\n"
        "v_fma_f32 v19, v19, s52, v118\n"
        "v_fma_f32 v25, v25, s52, v119\n"
        "v_mfma_f32_16x16x32_bf16 v[64:67], v[160:163], v[96:99], v[64:67]\n"
        "v_exp_f32 v19, v19\n"
        "v_mfma_f32_16x16x32_bf16 v[64:67], v[176:179], v[88:91], v[64:67]\n"
        "v_exp_f32 v24, v24\n"
        "v_mfma_f32_16x16x32_bf16 v[64:67], v[192:195], v[92:95], v[64:67]\n"
        "ds_read_b128 v[84:87], v110 offset:3072\n"
        "v_cvt_pk_bf16_f32 v17, v18, v19\n"
        "v_mfma_f32_16x16x32_bf16 v[68:71], v[164:167], v[96:99], v[68:71]\n"
        "ds_read_b128 v[104:107], v110 offset:4096\n"
        "v_fma_f32 v20, v20, s52, v118\n"
        "v_mfma_f32_16x16x32_bf16 v[68:71], v[180:183], v[88:91], v[68:71]\n"
        "ds_read_b128 v[80:83], v110 offset:5120\n"
        "v_fma_f32 v26, v26, s52, v119\n"
        "v_mfma_f32_16x16x32_bf16 v[68:71], v[196:199], v[92:95], v[68:71]\n"
        "v_exp_f32 v25, v25\n"
        "v_mfma_f32_16x16x32_bf16 v[72:75], v[168:171], v[96:99], v[72:75]\n"
        "v_cvt_pk_bf16_f32 v24, v24, v25\n"
        "v_fma_f32 v27, v27, s52, v119\n"
        "v_mfma_f32_16x16x32_bf16 v[72:75], v[184:187], v[88:91], v[72:75]\n"
        "v_exp_f32 v26, v26\n"
        "v_mfma_f32_16x16x32_bf16 v[72:75], v[200:203], v[92:95], v[72:75]\n"
        "v_exp_f32 v20, v20\n"
        "v_mfma_f32_16x16x32_bf16 v[76:79], v[172:175], v[96:99], v[76:79]\n"
        "v_exp_f32 v27, v27\n"
        "v_mfma_f32_16x16x32_bf16 v[76:79], v[188:191], v[88:91], v[76:79]\n"
        "v_fma_f32 v21, v21, s52, v118\n"
        "v_cvt_pk_bf16_f32 v25, v26, v27\n"
        "v_mfma_f32_16x16x32_bf16 v[76:79], v[204:207], v[92:95], v[76:79]\n"
        "s_waitcnt lgkmcnt(0)\n"
        "v_exp_f32 v21, v21\n"
        "v_mfma_f32_16x16x32_bf16 v[64:67], v[208:211], v[84:87], v[64:67]\n"
        "v_fma_f32 v28, v28, s52, v119\n"
        "v_cvt_pk_bf16_f32 v18, v20, v21\n"
        "v_mfma_f32_16x16x32_bf16 v[64:67], v[224:227], v[104:107], v[64:67]\n"
        "v_exp_f32 v28, v28\n"
        "v_mfma_f32_16x16x32_bf16 v[64:67], v[240:243], v[80:83], v[64:67]\n"
        "v_fma_f32 v29, v29, s52, v119\n"
        "v_fma_f32 v22, v22, s52, v118\n"
        "v_mfma_f32_16x16x32_bf16 v[68:71], v[212:215], v[84:87], v[68:71]\n"
        "v_exp_f32 v29, v29\n"
        "v_mfma_f32_16x16x32_bf16 v[68:71], v[228:231], v[104:107], v[68:71]\n"
        "v_cvt_pk_bf16_f32 v26, v28, v29\n"
        "v_fma_f32 v30, v30, s52, v119\n"
        "v_mfma_f32_16x16x32_bf16 v[68:71], v[244:247], v[80:83], v[68:71]\n"
        "v_exp_f32 v22, v22\n"
        "v_mfma_f32_16x16x32_bf16 v[72:75], v[216:219], v[84:87], v[72:75]\n"
        "v_fma_f32 v23, v23, s52, v118\n"
        "v_fma_f32 v31, v31, s52, v119\n"
        "v_mfma_f32_16x16x32_bf16 v[72:75], v[232:235], v[104:107], v[72:75]\n"
        "v_exp_f32 v30, v30\n"
        "v_mfma_f32_16x16x32_bf16 v[72:75], v[248:251], v[80:83], v[72:75]\n"
        "v_exp_f32 v23, v23\n"
        "v_mfma_f32_16x16x32_bf16 v[76:79], v[220:223], v[84:87], v[76:79]\n"
        "v_cvt_pk_bf16_f32 v19, v22, v23\n"
        "v_fma_f32 v32, v32, s52, v119\n"
        "v_mfma_f32_16x16x32_bf16 v[76:79], v[236:239], v[104:107], v[76:79]\n"
        "ds_write_b128 v109, v[16:19] offset:2048\n"
        "v_mfma_f32_16x16x32_bf16 v[76:79], v[252:255], v[80:83], v[76:79]\n"
        "ds_read_b128 v[104:107], v109 offset:0\n"
        "v_fma_f32 v33, v33, s52, v119\n"
        "v_mfma_f32_16x16x32_bf16 v[80:83], v[124:127], v[0:3], 0\n"
        "ds_read_b128 v[84:87], v109 offset:1024\n"
        "v_fma_f32 v34, v34, s52, v119\n"
        "v_mfma_f32_16x16x32_bf16 v[80:83], v[124:127], v[8:11], v[80:83]\n"
        "ds_read_b128 v[92:95], v109 offset:2048\n"
        "v_fma_f32 v35, v35, s52, v119\n"
        "v_mfma_f32_16x16x32_bf16 v[80:83], v[124:127], v[16:19], v[80:83]\n"
        "v_exp_f32 v31, v31\n"
        "s_nop 0\n"
        "v_cvt_pk_bf16_f32 v27, v30, v31\n"
        "v_exp_f32 v32, v32\n"
        "v_fma_f32 v36, v36, s52, v119\n"
        "ds_write_b128 v109, v[24:27] offset:3072\n"
        "s_waitcnt lgkmcnt(2)\n"
        "v_exp_f32 v33, v33\n"
        "v_exp_f32 v34, v34\n"
        "v_mfma_f32_16x16x32_bf16 v[0:3], a[192:195], v[104:107], 0\n"
        "s_waitcnt lgkmcnt(1)\n"
        "ds_write_b32 v113, v80 offset:512\n"
        "v_mfma_f32_16x16x32_bf16 v[0:3], a[208:211], v[84:87], v[0:3]\n"
        "v_cvt_pk_bf16_f32 v32, v32, v33\n"
        "v_fma_f32 v37, v37, s52, v119\n"
        "v_mfma_f32_16x16x32_bf16 v[0:3], a[224:227], v[92:95], v[0:3]\n"
        "v_exp_f32 v35, v35\n"
        "v_mfma_f32_16x16x32_bf16 v[4:7], a[196:199], v[104:107], 0\n"
        "v_cvt_pk_bf16_f32 v33, v34, v35\n"
        "v_fma_f32 v38, v38, s52, v119\n"
        "v_mfma_f32_16x16x32_bf16 v[4:7], a[212:215], v[84:87], v[4:7]\n"
        "v_exp_f32 v36, v36\n"
        "v_mfma_f32_16x16x32_bf16 v[4:7], a[228:231], v[92:95], v[4:7]\n"
        "v_exp_f32 v37, v37\n"
        "v_mfma_f32_16x16x32_bf16 v[8:11], a[200:203], v[104:107], 0\n"
        "v_cvt_pk_bf16_f32 v34, v36, v37\n"
        "v_fma_f32 v39, v39, s52, v119\n"
        "v_mfma_f32_16x16x32_bf16 v[8:11], a[216:219], v[84:87], v[8:11]\n"
        "v_exp_f32 v38, v38\n"
        "v_mfma_f32_16x16x32_bf16 v[8:11], a[232:235], v[92:95], v[8:11]\n"
        "v_exp_f32 v39, v39\n"
        "v_mfma_f32_16x16x32_bf16 v[12:15], a[204:207], v[104:107], 0\n"
        "v_fma_f32 v40, v40, s52, v119\n"
        "v_exp_f32 v40, v40\n"
        "v_fma_f32 v41, v41, s52, v119\n"
        "v_exp_f32 v41, v41\n"
        "v_fma_f32 v42, v42, s52, v119\n"
        "v_exp_f32 v42, v42\n"
        "v_fma_f32 v43, v43, s52, v119\n"
        "v_exp_f32 v43, v43\n"
        "v_fma_f32 v44, v44, s52, v119\n"
        "v_exp_f32 v44, v44\n"
        "v_fma_f32 v45, v45, s52, v119\n"
        "v_exp_f32 v45, v45\n"
        "v_fma_f32 v46, v46, s52, v119\n"
        "v_exp_f32 v46, v46\n"
        "v_fma_f32 v47, v47, s52, v119\n"
        "v_exp_f32 v47, v47\n"
        "v_mfma_f32_16x16x32_bf16 v[12:15], a[220:223], v[84:87], v[12:15]\n"
        "v_cvt_pk_bf16_f32 v35, v38, v39\n"
        "v_cvt_pk_bf16_f32 v40, v40, v41\n"
        "v_cvt_pk_bf16_f32 v41, v42, v43\n"
        "v_cvt_pk_bf16_f32 v42, v44, v45\n"
        "v_cvt_pk_bf16_f32 v43, v46, v47\n"
        "v_mfma_f32_16x16x32_bf16 v[12:15], a[236:239], v[92:95], v[12:15]\n"
        "ds_write_b128 v109, v[32:35] offset:4096\n"
        "v_mfma_f32_16x16x32_bf16 v[92:95], v[124:127], v[24:27], 0\n"
        "v_add_u32 v110, s57, v109\n"
        "v_xor_b32 v112, 0x8000, v112\n"
        "v_mfma_f32_16x16x32_bf16 v[92:95], v[124:127], v[32:35], v[92:95]\n"
        "v_mfma_f32_16x16x32_bf16 v[92:95], v[124:127], v[40:43], v[92:95]\n"
        "ds_write_b128 v109, v[40:43] offset:5120\n"
        "ds_read_b128 v[80:83], v109 offset:3072\n"
        "s_nop 7\n"
        "ds_write_b32 v113, v92 offset:768\n"
        "ds_read_b128 v[84:87], v109 offset:4096\n"
        "ds_read_b128 v[88:91], v109 offset:5120\n"
        "s_waitcnt lgkmcnt(0)\n"
        "s_barrier\n"
        "v_mfma_f32_16x16x32_bf16 v[24:27], a[192:195], v[80:83], 0\n"
        "v_mul_f32 v48, v120, v48\n"
        "v_mul_f32 v49, v120, v49\n"
        "v_mfma_f32_16x16x32_bf16 v[24:27], a[208:211], v[84:87], v[24:27]\n"
        "v_mul_f32 v50, v120, v50\n"
        "v_mul_f32 v51, v120, v51\n"
        "v_mfma_f32_16x16x32_bf16 v[24:27], a[224:227], v[88:91], v[24:27]\n"
        "ds_read_b128 v[92:95], v110 offset:0\n"
        "v_cvt_pk_bf16_f32 v48, v48, v49\n"
        "v_mfma_f32_16x16x32_bf16 v[28:31], a[196:199], v[80:83], 0\n"
        "ds_read_b128 v[96:99], v110 offset:1024\n"
        "v_cvt_pk_bf16_f32 v49, v50, v51\n"
        "v_mfma_f32_16x16x32_bf16 v[28:31], a[212:215], v[84:87], v[28:31]\n"
        "ds_read_b128 v[100:103], v110 offset:2048\n"
        "v_add_u32 v110, s58, v110\n"
        "v_mfma_f32_16x16x32_bf16 v[28:31], a[228:231], v[88:91], v[28:31]\n"
        "v_mul_f32 v52, v120, v52\n"
        "v_mul_f32 v53, v120, v53\n"
        "v_mfma_f32_16x16x32_bf16 v[32:35], a[200:203], v[80:83], 0\n"
        "ds_write_b64 v114, v[48:49] offset:0\n"
        "v_mfma_f32_16x16x32_bf16 v[32:35], a[216:219], v[84:87], v[32:35]\n"
        "v_mul_f32 v54, v120, v54\n"
        "v_mul_f32 v55, v120, v55\n"
        "v_mfma_f32_16x16x32_bf16 v[32:35], a[232:235], v[88:91], v[32:35]\n"
        "v_cvt_pk_bf16_f32 v52, v52, v53\n"
        "v_cvt_pk_bf16_f32 v53, v54, v55\n"
        "v_mfma_f32_16x16x32_bf16 v[36:39], a[204:207], v[80:83], 0\n"
        "ds_write_b64 v114, v[52:53] offset:32\n"
        "v_mfma_f32_16x16x32_bf16 v[36:39], a[220:223], v[84:87], v[36:39]\n"
        "v_mul_f32 v56, v120, v56\n"
        "v_mul_f32 v57, v120, v57\n"
        "v_mfma_f32_16x16x32_bf16 v[36:39], a[236:239], v[88:91], v[36:39]\n"
        "s_waitcnt lgkmcnt(2)\n"
        "v_mul_f32 v58, v120, v58\n"
        "v_mul_f32 v59, v120, v59\n"
        "v_mfma_f32_16x16x32_bf16 v[0:3], a[240:243], v[92:95], v[0:3]\n"
        "v_cvt_pk_bf16_f32 v56, v56, v57\n"
        "v_mfma_f32_16x16x32_bf16 v[0:3], v[128:131], v[96:99], v[0:3]\n"
        "v_cvt_pk_bf16_f32 v57, v58, v59\n"
        "v_mul_f32 v60, v120, v60\n"
        "v_mfma_f32_16x16x32_bf16 v[0:3], v[144:147], v[100:103], v[0:3]\n"
        "ds_read_b128 v[88:91], v110 offset:0\n"
        "v_mul_f32 v61, v120, v61\n"
        "v_mfma_f32_16x16x32_bf16 v[4:7], a[244:247], v[92:95], v[4:7]\n"
        "ds_read_b128 v[84:87], v110 offset:1024\n"
        "v_mul_f32 v62, v120, v62\n"
        "v_mfma_f32_16x16x32_bf16 v[4:7], v[132:135], v[96:99], v[4:7]\n"
        "ds_read_b128 v[80:83], v110 offset:2048\n"
        "v_add_u32 v110, s59, v110\n"
        "v_mfma_f32_16x16x32_bf16 v[4:7], v[148:151], v[100:103], v[4:7]\n"
        "v_mul_f32 v63, v120, v63\n"
        "v_cvt_pk_bf16_f32 v60, v60, v61\n"
        "v_mfma_f32_16x16x32_bf16 v[8:11], a[248:251], v[92:95], v[8:11]\n"
        "ds_write_b64 v114, v[56:57] offset:64\n"
        "v_mfma_f32_16x16x32_bf16 v[8:11], v[136:139], v[96:99], v[8:11]\n"
        "v_cvt_pk_bf16_f32 v61, v62, v63\n"
        "v_mul_f32 v64, v121, v64\n"
        "v_mfma_f32_16x16x32_bf16 v[8:11], v[152:155], v[100:103], v[8:11]\n"
        "ds_write_b64 v114, v[60:61] offset:96\n"
        "v_mfma_f32_16x16x32_bf16 v[12:15], a[252:255], v[92:95], v[12:15]\n"
        "v_mul_f32 v65, v121, v65\n"
        "v_mul_f32 v66, v121, v66\n"
        "v_mfma_f32_16x16x32_bf16 v[12:15], v[140:143], v[96:99], v[12:15]\n"
        "ds_read_b128 v[48:51], v115 offset:0\n"
        "ds_read_b128 v[52:55], v115 offset:1152\n"
        "v_mfma_f32_16x16x32_bf16 v[12:15], v[156:159], v[100:103], v[12:15]\n"
        "s_waitcnt lgkmcnt(4)\n"
        "ds_read_b128 v[100:103], v112 offset:512\n"
        "v_mul_f32 v67, v121, v67\n"
        "v_mfma_f32_16x16x32_bf16 v[0:3], v[160:163], v[88:91], v[0:3]\n"
        "v_cvt_pk_bf16_f32 v64, v64, v65\n"
        "v_cvt_pk_bf16_f32 v65, v66, v67\n"
        "v_mfma_f32_16x16x32_bf16 v[0:3], v[176:179], v[84:87], v[0:3]\n"
        "v_mul_f32 v68, v121, v68\n"
        "v_mul_f32 v69, v121, v69\n"
        "v_mfma_f32_16x16x32_bf16 v[0:3], v[192:195], v[80:83], v[0:3]\n"
        "ds_read_b128 v[96:99], v110 offset:0\n"
        "v_mul_f32 v70, v121, v70\n"
        "v_mfma_f32_16x16x32_bf16 v[4:7], v[164:167], v[88:91], v[4:7]\n"
        "s_waitcnt lgkmcnt(3)\n"
        "ds_read_b128 v[92:95], v110 offset:1024\n"
        "v_mul_f32 v71, v121, v71\n"
        "v_mfma_f32_16x16x32_bf16 v[4:7], v[180:183], v[84:87], v[4:7]\n"
        "s_waitcnt lgkmcnt(3)\n"
        "ds_read_b128 v[104:107], v110 offset:2048\n"
        "v_add_u32 v110, s60, v110\n"
        "v_mfma_f32_16x16x32_bf16 v[4:7], v[196:199], v[80:83], v[4:7]\n"
        "s_waitcnt lgkmcnt(3)\n"
        "buffer_store_dwordx4 v[48:51], v116, s[48:51], s56 offen sc1\n"
        "v_mfma_f32_16x16x32_bf16 v[8:11], v[168:171], v[88:91], v[8:11]\n"
        "s_add_u32 s56, s56, 4096\n"
        "ds_write_b64 v114, v[64:65] offset:2304\n"
        "v_mfma_f32_16x16x32_bf16 v[8:11], v[184:187], v[84:87], v[8:11]\n"
        "buffer_store_dwordx4 v[52:55], v116, s[48:51], s56 offen sc1\n"
        "v_mfma_f32_16x16x32_bf16 v[8:11], v[200:203], v[80:83], v[8:11]\n"
        "s_add_u32 s56, s56, 4096\n"
        "v_cvt_pk_bf16_f32 v68, v68, v69\n"
        "v_cvt_pk_bf16_f32 v69, v70, v71\n"
        "v_mfma_f32_16x16x32_bf16 v[12:15], v[172:175], v[88:91], v[12:15]\n"
        "v_add_f32 v118, v100, v101\n"
        "v_add_f32 v102, v102, v103\n"
        "v_add_f32 v118, v118, v102\n"
        "v_mfma_f32_16x16x32_bf16 v[12:15], v[188:191], v[84:87], v[12:15]\n"
        "v_mul_f32 v72, v121, v72\n"
        "v_mul_f32 v73, v121, v73\n"
        "v_mfma_f32_16x16x32_bf16 v[12:15], v[204:207], v[80:83], v[12:15]\n"
        "s_waitcnt lgkmcnt(1)\n"
        "ds_read_b128 v[80:83], v112 offset:768\n"
        "v_mul_f32 v74, v121, v74\n"
        "v_mfma_f32_16x16x32_bf16 v[0:3], v[208:211], v[96:99], v[0:3]\n"
        "ds_write_b64 v114, v[68:69] offset:2336\n"
        "v_mfma_f32_16x16x32_bf16 v[0:3], v[224:227], v[92:95], v[0:3]\n"
        "v_mul_f32 v75, v121, v75\n"
        "v_cvt_pk_bf16_f32 v72, v72, v73\n"
        "v_mfma_f32_16x16x32_bf16 v[0:3], v[240:243], v[104:107], v[0:3]\n"
        "ds_read_b128 v[84:87], v110 offset:3072\n"
        "v_cvt_pk_bf16_f32 v73, v74, v75\n"
        "v_mfma_f32_16x16x32_bf16 v[4:7], v[212:215], v[96:99], v[4:7]\n"
        "ds_read_b128 v[100:103], v110 offset:4096\n"
        "v_rcp_f32 v118, v118\n"
        "v_mfma_f32_16x16x32_bf16 v[4:7], v[228:231], v[92:95], v[4:7]\n"
        "ds_read_b128 v[88:91], v110 offset:5120\n"
        "v_add_u32 v110, s58, v110\n"
        "v_mfma_f32_16x16x32_bf16 v[4:7], v[244:247], v[104:107], v[4:7]\n"
        "s_waitcnt lgkmcnt(4)\n"
        "v_mul_f32 v76, v121, v76\n"
        "v_mul_f32 v77, v121, v77\n"
        "v_mfma_f32_16x16x32_bf16 v[8:11], v[216:219], v[96:99], v[8:11]\n"
        "ds_write_b64 v114, v[72:73] offset:2368\n"
        "v_mfma_f32_16x16x32_bf16 v[8:11], v[232:235], v[92:95], v[8:11]\n"
        "v_mul_f32 v78, v121, v78\n"
        "v_mul_f32 v79, v121, v79\n"
        "v_mfma_f32_16x16x32_bf16 v[8:11], v[248:251], v[104:107], v[8:11]\n"
        "v_cvt_pk_bf16_f32 v76, v76, v77\n"
        "v_cvt_pk_bf16_f32 v77, v78, v79\n"
        "v_mfma_f32_16x16x32_bf16 v[12:15], v[220:223], v[96:99], v[12:15]\n"
        "v_add_f32 v119, v80, v81\n"
        "v_add_f32 v82, v82, v83\n"
        "v_add_f32 v119, v119, v82\n"
        "v_mfma_f32_16x16x32_bf16 v[12:15], v[236:239], v[92:95], v[12:15]\n"
        "ds_write_b64 v114, v[76:77] offset:2400\n"
        "v_mfma_f32_16x16x32_bf16 v[12:15], v[252:255], v[104:107], v[12:15]\n"
        "s_waitcnt lgkmcnt(2)\n"
        "v_mul_f32 v0, v118, v0\n"
        "v_mul_f32 v1, v118, v1\n"
        "v_mfma_f32_16x16x32_bf16 v[24:27], a[240:243], v[84:87], v[24:27]\n"
        "ds_read_b128 v[64:67], v115 offset:2304\n"
        "ds_read_b128 v[68:71], v115 offset:3456\n"
        "v_mfma_f32_16x16x32_bf16 v[24:27], v[128:131], v[100:103], v[24:27]\n"
        "v_mul_f32 v2, v118, v2\n"
        "v_mul_f32 v3, v118, v3\n"
        "v_mfma_f32_16x16x32_bf16 v[24:27], v[144:147], v[88:91], v[24:27]\n"
        "ds_read_b128 v[104:107], v110 offset:3072\n"
        "v_cvt_pk_bf16_f32 v0, v0, v1\n"
        "v_mfma_f32_16x16x32_bf16 v[28:31], a[244:247], v[84:87], v[28:31]\n"
        "ds_read_b128 v[80:83], v110 offset:4096\n"
        "v_rcp_f32 v119, v119\n"
        "v_mfma_f32_16x16x32_bf16 v[28:31], v[132:135], v[100:103], v[28:31]\n"
        "ds_read_b128 v[92:95], v110 offset:5120\n"
        "v_add_u32 v110, s59, v110\n"
        "v_mfma_f32_16x16x32_bf16 v[28:31], v[148:151], v[88:91], v[28:31]\n"
        "s_waitcnt lgkmcnt(4)\n"
        "buffer_store_dwordx4 v[64:67], v116, s[48:51], s56 offen sc1\n"
        "v_mfma_f32_16x16x32_bf16 v[32:35], a[248:251], v[84:87], v[32:35]\n"
        "s_waitcnt lgkmcnt(3)\n"
        "v_cvt_pk_bf16_f32 v1, v2, v3\n"
        "v_mul_f32 v4, v118, v4\n"
        "v_mfma_f32_16x16x32_bf16 v[32:35], v[136:139], v[100:103], v[32:35]\n"
        "s_add_u32 s56, s56, 4096\n"
        "ds_write_b64 v114, v[0:1] offset:0\n"
        "v_mfma_f32_16x16x32_bf16 v[32:35], v[152:155], v[88:91], v[32:35]\n"
        "buffer_store_dwordx4 v[68:71], v116, s[48:51], s56 offen sc1\n"
        "v_mfma_f32_16x16x32_bf16 v[36:39], a[252:255], v[84:87], v[36:39]\n"
        "s_add_u32 s56, s56, 4096\n"
        "v_mul_f32 v5, v118, v5\n"
        "v_mul_f32 v6, v118, v6\n"
        "v_mfma_f32_16x16x32_bf16 v[36:39], v[140:143], v[100:103], v[36:39]\n"
        "v_mul_f32 v7, v118, v7\n"
        "v_cvt_pk_bf16_f32 v4, v4, v5\n"
        "v_mfma_f32_16x16x32_bf16 v[36:39], v[156:159], v[88:91], v[36:39]\n"
        "s_waitcnt lgkmcnt(1)\n"
        "v_cvt_pk_bf16_f32 v5, v6, v7\n"
        "v_mfma_f32_16x16x32_bf16 v[24:27], v[160:163], v[104:107], v[24:27]\n"
        "ds_write_b64 v114, v[4:5] offset:32\n"
        "v_mfma_f32_16x16x32_bf16 v[24:27], v[176:179], v[80:83], v[24:27]\n"
        "v_mul_f32 v8, v118, v8\n"
        "v_mul_f32 v9, v118, v9\n"
        "v_mfma_f32_16x16x32_bf16 v[24:27], v[192:195], v[92:95], v[24:27]\n"
        "ds_read_b128 v[88:91], v110 offset:3072\n"
        "v_mul_f32 v10, v118, v10\n"
        "v_mfma_f32_16x16x32_bf16 v[28:31], v[164:167], v[104:107], v[28:31]\n"
        "ds_read_b128 v[100:103], v110 offset:4096\n"
        "v_mul_f32 v11, v118, v11\n"
        "v_mfma_f32_16x16x32_bf16 v[28:31], v[180:183], v[80:83], v[28:31]\n"
        "ds_read_b128 v[84:87], v110 offset:5120\n"
        "v_cvt_pk_bf16_f32 v8, v8, v9\n"
        "v_mfma_f32_16x16x32_bf16 v[28:31], v[196:199], v[92:95], v[28:31]\n"
        "v_cvt_pk_bf16_f32 v9, v10, v11\n"
        "v_mul_f32 v12, v118, v12\n"
        "v_mfma_f32_16x16x32_bf16 v[32:35], v[168:171], v[104:107], v[32:35]\n"
        "ds_write_b64 v114, v[8:9] offset:64\n"
        "v_mfma_f32_16x16x32_bf16 v[32:35], v[184:187], v[80:83], v[32:35]\n"
        "v_mul_f32 v13, v118, v13\n"
        "v_mul_f32 v14, v118, v14\n"
        "v_mfma_f32_16x16x32_bf16 v[32:35], v[200:203], v[92:95], v[32:35]\n"
        "v_mul_f32 v15, v118, v15\n"
        "v_cvt_pk_bf16_f32 v12, v12, v13\n"
        "v_mfma_f32_16x16x32_bf16 v[36:39], v[172:175], v[104:107], v[36:39]\n"
        "v_cvt_pk_bf16_f32 v13, v14, v15\n"
        "v_mfma_f32_16x16x32_bf16 v[36:39], v[188:191], v[80:83], v[36:39]\n"
        "ds_write_b64 v114, v[12:13] offset:96\n"
        "v_mfma_f32_16x16x32_bf16 v[36:39], v[204:207], v[92:95], v[36:39]\n"
        "s_waitcnt lgkmcnt(2)\n"
        "v_mfma_f32_16x16x32_bf16 v[24:27], v[208:211], v[88:91], v[24:27]\n"
        "ds_read_b128 v[0:3], v115 offset:0\n"
        "ds_read_b128 v[4:7], v115 offset:1152\n"
        "v_mfma_f32_16x16x32_bf16 v[24:27], v[224:227], v[100:103], v[24:27]\n"
        "v_mfma_f32_16x16x32_bf16 v[24:27], v[240:243], v[84:87], v[24:27]\n"
        "v_mfma_f32_16x16x32_bf16 v[28:31], v[212:215], v[88:91], v[28:31]\n"
        "v_mfma_f32_16x16x32_bf16 v[28:31], v[228:231], v[100:103], v[28:31]\n"
        "v_mfma_f32_16x16x32_bf16 v[28:31], v[244:247], v[84:87], v[28:31]\n"
        "s_waitcnt lgkmcnt(1)\n"
        "buffer_store_dwordx4 v[0:3], v116, s[48:51], s56 offen sc1\n"
        "v_mfma_f32_16x16x32_bf16 v[32:35], v[216:219], v[88:91], v[32:35]\n"
        "s_waitcnt lgkmcnt(0)\n"
        "v_mul_f32 v24, v119, v24\n"
        "v_mul_f32 v25, v119, v25\n"
        "v_mfma_f32_16x16x32_bf16 v[32:35], v[232:235], v[100:103], v[32:35]\n"
        "s_add_u32 s56, s56, 4096\n"
        "v_mul_f32 v26, v119, v26\n"
        "v_mul_f32 v27, v119, v27\n"
        "v_mfma_f32_16x16x32_bf16 v[32:35], v[248:251], v[84:87], v[32:35]\n"
        "buffer_store_dwordx4 v[4:7], v116, s[48:51], s56 offen sc1\n"
        "v_mfma_f32_16x16x32_bf16 v[36:39], v[220:223], v[88:91], v[36:39]\n"
        "s_add_u32 s56, s56, 4096\n"
        "v_cvt_pk_bf16_f32 v24, v24, v25\n"
        "v_cvt_pk_bf16_f32 v25, v26, v27\n"
        "v_mfma_f32_16x16x32_bf16 v[36:39], v[236:239], v[100:103], v[36:39]\n"
        "ds_write_b64 v114, v[24:25] offset:2304\n"
        "v_mfma_f32_16x16x32_bf16 v[36:39], v[252:255], v[84:87], v[36:39]\n"
        "v_mul_f32 v28, v119, v28\n"
        "v_mul_f32 v29, v119, v29\n"
        "v_mul_f32 v30, v119, v30\n"
        "v_mul_f32 v31, v119, v31\n"
        "v_cvt_pk_bf16_f32 v28, v28, v29\n"
        "v_cvt_pk_bf16_f32 v29, v30, v31\n"
        "ds_write_b64 v114, v[28:29] offset:2336\n"
        "v_mul_f32 v32, v119, v32\n"
        "v_mul_f32 v33, v119, v33\n"
        "v_mul_f32 v34, v119, v34\n"
        "v_mul_f32 v35, v119, v35\n"
        "v_cvt_pk_bf16_f32 v32, v32, v33\n"
        "v_cvt_pk_bf16_f32 v33, v34, v35\n"
        "v_mul_f32 v36, v119, v36\n"
        "v_mul_f32 v37, v119, v37\n"
        "v_mul_f32 v38, v119, v38\n"
        "ds_write_b64 v114, v[32:33] offset:2368\n"
        "v_mul_f32 v39, v119, v39\n"
        "v_cvt_pk_bf16_f32 v36, v36, v37\n"
        "v_cvt_pk_bf16_f32 v37, v38, v39\n"
        "ds_write_b64 v114, v[36:37] offset:2400\n"
        "ds_read_b128 v[24:27], v115 offset:2304\n"
        "ds_read_b128 v[28:31], v115 offset:3456\n"
        "s_waitcnt lgkmcnt(1)\n"
        "buffer_store_dwordx4 v[24:27], v116, s[48:51], s56 offen sc1\n"
        "s_waitcnt lgkmcnt(0)\n"
        "s_add_u32 s56, s56, 4096\n"
        "buffer_store_dwordx4 v[28:31], v116, s[48:51], s56 offen sc1\n"
        "s_add_u32 s56, s56, 4096\n"
        "s_endpgm\n"
        "9:\n"
        :
        : "s"(q), "s"(k), "s"(v), "s"(o), "s"(blockIdx.x), "s"(wave), "s"((unsigned)(size_t)lds)
        : "v0", "v1", "v2", "v3", "v4", "v5", "v6", "v7", "v8", "v9", "v10", "v11", "v12", "v13", "v14", "v15", "v16", "v17", "v18", "v19", "v20", "v21", "v22", "v23", "v24", "v25", "v26", "v27", "v28", "v29", "v30", "v31", "v32", "v33", "v34", "v35", "v36", "v37", "v38", "v39", "v40", "v41", "v42", "v43", "v44", "v45", "v46", "v47", "v48", "v49", "v50", "v51", "v52", "v53", "v54", "v55", "v56", "v57", "v58", "v59", "v60", "v61", "v62", "v63", "v64", "v65", "v66", "v67", "v68", "v69", "v70", "v71", "v72", "v73", "v74", "v75", "v76", "v77", "v78", "v79", "v80", "v81", "v82", "v83", "v84", "v85", "v86", "v87", "v88", "v89", "v90", "v91", "v92", "v93", "v94", "v95", "v96", "v97", "v98", "v99", "v100", "v101", "v102", "v103", "v104", "v105", "v106", "v107", "v108", "v109", "v110", "v111", "v112", "v113", "v114", "v115", "v116", "v117", "v118", "v119", "v120", "v121", "v122", "v123", "v124", "v125", "v126", "v127", "v128", "v129", "v130", "v131", "v132", "v133", "v134", "v135", "v136", "v137", "v138", "v139", "v140", "v141", "v142", "v143", "v144", "v145", "v146", "v147", "v148", "v149", "v150", "v151", "v152", "v153", "v154", "v155", "v156", "v157", "v158", "v159", "v160", "v161", "v162", "v163", "v164", "v165", "v166", "v167", "v168", "v169", "v170", "v171", "v172", "v173", "v174", "v175", "v176", "v177", "v178", "v179", "v180", "v181", "v182", "v183", "v184", "v185", "v186", "v187", "v188", "v189", "v190", "v191", "v192", "v193", "v194", "v195", "v196", "v197", "v198", "v199", "v200", "v201", "v202", "v203", "v204", "v205", "v206", "v207", "v208", "v209", "v210", "v211", "v212", "v213", "v214", "v215", "v216", "v217", "v218", "v219", "v220", "v221", "v222", "v223", "v224", "v225", "v226", "v227", "v228", "v229", "v230", "v231", "v232", "v233", "v234", "v235", "v236", "v237", "v238", "v239", "v240", "v241", "v242", "v243", "v244", "v245", "v246", "v247", "v248", "v249", "v250", "v251", "v252", "v253", "v254", "v255", "a0", "a1", "a2", "a3", "a4", "a5", "a6", "a7", "a8", "a9", "a10", "a11", "a12", "a13", "a14", "a15", "a16", "a17", "a18", "a19", "a20", "a21", "a22", "a23", "a24", "a25", "a26", "a27", "a28", "a29", "a30", "a31", "a32", "a33", "a34", "a35", "a36", "a37", "a38", "a39", "a40", "a41", "a42", "a43", "a44", "a45", "a46", "a47", "a48", "a49", "a50", "a51", "a52", "a53", "a54", "a55", "a56", "a57", "a58", "a59", "a60", "a61", "a62", "a63", "a64", "a65", "a66", "a67", "a68", "a69", "a70", "a71", "a72", "a73", "a74", "a75", "a76", "a77", "a78", "a79", "a80", "a81", "a82", "a83", "a84", "a85", "a86", "a87", "a88", "a89", "a90", "a91", "a92", "a93", "a94", "a95", "a96", "a97", "a98", "a99", "a100", "a101", "a102", "a103", "a104", "a105", "a106", "a107", "a108", "a109", "a110", "a111", "a112", "a113", "a114", "a115", "a116", "a117", "a118", "a119", "a120", "a121", "a122", "a123", "a124", "a125", "a126", "a127", "a128", "a129", "a130", "a131", "a132", "a133", "a134", "a135", "a136", "a137", "a138", "a139", "a140", "a141", "a142", "a143", "a144", "a145", "a146", "a147", "a148", "a149", "a150", "a151", "a152", "a153", "a154", "a155", "a156", "a157", "a158", "a159", "a160", "a161", "a162", "a163", "a164", "a165", "a166", "a167", "a168", "a169", "a170", "a171", "a172", "a173", "a174", "a175", "a176", "a177", "a178", "a179", "a180", "a181", "a182", "a183", "a184", "a185", "a186", "a187", "a188", "a189", "a190", "a191", "a192", "a193", "a194", "a195", "a196", "a197", "a198", "a199", "a200", "a201", "a202", "a203", "a204", "a205", "a206", "a207", "a208", "a209", "a210", "a211", "a212", "a213", "a214", "a215", "a216", "a217", "a218", "a219", "a220", "a221", "a222", "a223", "a224", "a225", "a226", "a227", "a228", "a229", "a230", "a231", "a232", "a233", "a234", "a235", "a236", "a237", "a238", "a239", "a240", "a241", "a242", "a243", "a244", "a245", "a246", "a247", "a248", "a249", "a250", "a251", "a252", "a253", "a254", "a255", "s36", "s37", "s38", "s39", "s40", "s41", "s42", "s43", "s44", "s45", "s46", "s47", "s48", "s49", "s50", "s51", "s52", "s53", "s54", "s55", "s56", "s57", "s58", "s59", "s60", "s61", "s62", "s63", "s64", "s65", "s66", "s67", "s68", "s69", "s70", "s71", "s72", "s73", "s74", "s75", "s76", "s77", "s78", "s79", "s80", "s81", "s82", "s83", "s84", "s85", "s86", "s87", "s88", "s89", "s90", "s91", "s92", "s93", "s94", "s95", "s96", "s97", "s98", "s99", "vcc", "scc", "memory");
}

extern "C" void kernel_launch(void* const* d_in, const int* in_sizes, int n_in, void* d_out, int out_size,
                              void* d_ws, size_t ws_size, hipStream_t stream) {
    gqa_softmax_kernel<<<256, 256, 0, stream>>>(d_in[0], d_in[1], d_in[2], d_out);
}
```
